```python
import torch

def _softmax(x, axis=-1):
    return torch.softmax(x, dim=axis)
def _gelu(x):
    return torch.nn.functional.gelu(x, approximate="tanh")
def _silu(x):
    return torch.nn.functional.silu(x)
def _sigmoid(x):
    return torch.sigmoid(x)
def _layernorm(x, axis=-1, eps=1e-5):
    m = x.mean(dim=axis, keepdim=True)
    v = x.var(dim=axis, keepdim=True, unbiased=False)
    return (x - m) / torch.sqrt(v + eps)
def _scatter_add(tgt, idx, src, axis=0):
    return tgt.clone().index_add_(axis, idx, src)
def _segment_sum(src, seg, n):
    out = torch.zeros((n,) + src.shape[1:], dtype=src.dtype)
    return out.index_add_(0, seg, src)
def _pad(x, pw):
    flat = tuple(v for pair in reversed(pw) for v in pair)
    return torch.nn.functional.pad(x, flat)

kernel_name = 'standalone__gqa_block_cached__near_ridge'

def setup_inputs():

    _w = (lambda a: (lambda t: t.to(torch.bfloat16) if t.is_floating_point() else t)(torch.as_tensor(a)))
    B, Sq, D = 32, 144, 640
    Hq, Hkv, Sk, Dh = 32, 32, 512, 128
    assert Hq % Hkv == 0, (Hq, Hkv)
    return {
        "x": _w((lambda *s: torch.randn(*s, dtype=torch.bfloat16) * 0.8)(B, Sq, D)),
        "k_cache": _w((lambda *s: torch.randn(*s, dtype=torch.bfloat16) * 0.8)(B, Hkv, Sk, Dh)),
        "v_cache": _w((lambda *s: torch.randn(*s, dtype=torch.bfloat16) * 0.05)(B, Hkv, Sk, Dh)),
        "wq": _w((lambda *s: torch.randn(*s, dtype=torch.bfloat16) * 0.8)(D, Hq * Dh) * (D ** -0.5)),
        "wo": _w((lambda *s: torch.randn(*s, dtype=torch.bfloat16) * 0.05)(Hq * Dh, D) * ((Hq * Dh) ** -0.5)),
    }

def reference(x, k_cache, v_cache, wq, wo):
    B, Sq, D = x.shape
    Hq, Dh = 32, 128
    Hkv = k_cache.shape[1]
    rep = Hq // Hkv
    q = (lambda x, *d: x.permute(*d))((x @ wq).reshape(B, Sq, Hq, Dh), 0, 2, 1, 3)
    k = (lambda x, n, axis: torch.repeat_interleave(x, n, dim=axis))(k_cache, rep, axis=1)
    v = (lambda x, n, axis: torch.repeat_interleave(x, n, dim=axis))(v_cache, rep, axis=1)
    scores = torch.einsum("bhqd,bhkd->bhqk", q, k) * (Dh ** -0.5)
    p = _softmax(scores, axis=-1)
    o = torch.einsum("bhqk,bhkd->bhqd", p, v)
    return (lambda x, *d: x.permute(*d))(o, 0, 2, 1, 3).reshape(B, Sq, Hq * Dh) @ wo
```

```cpp
#include <hip/hip_runtime.h>
#include <hip/hip_bf16.h>

typedef short    s16x4 __attribute__((ext_vector_type(4)));
typedef __bf16   bf16x8 __attribute__((ext_vector_type(8)));
typedef __bf16   bf16x2 __attribute__((ext_vector_type(2)));
typedef float    f32x4 __attribute__((ext_vector_type(4)));
typedef float    f32x2 __attribute__((ext_vector_type(2)));
typedef unsigned u32x4 __attribute__((ext_vector_type(4)));
typedef unsigned u32x2 __attribute__((ext_vector_type(2)));
typedef unsigned short u16;
#define LDSP __attribute__((address_space(3)))
#define DI __device__ __forceinline__
template <int N> struct IC { static constexpr int value = N; };

constexpr int NB = 32, SQ = 144, DM = 640, NH = 32, SK = 512, DH = 128;
constexpr float QSCALE = 0.08838834764831845f * 1.4426950408889634f;
constexpr float THR = 8.0f;

constexpr size_t WS_O  = 0;
constexpr size_t WS_WO = (size_t)NB * 128 * 9 * 1024;

constexpr unsigned L_Q9 = 65536, L_P9 = L_Q9 + 4096, L_M9 = L_P9 + 2048, L_X9 = L_M9 + 256, L_L9 = L_X9 + 256, L_XN = L_L9 + 512, L_END = L_XN + 3 * 2048;

DI f32x4 mfma(u32x4 a, u32x4 b, f32x4 c) {
  return __builtin_amdgcn_mfma_f32_16x16x32_bf16(__builtin_bit_cast(bf16x8, a), __builtin_bit_cast(bf16x8, b), c, 0, 0, 0);
}
DI unsigned cvtpk(float lo, float hi) {
  f32x2 v = {lo, hi};
  return __builtin_bit_cast(unsigned, __builtin_convertvector(v, bf16x2));
}
DI float ex2(float x) { return __builtin_amdgcn_exp2f(x); }
DI float xmax(float v) {
  unsigned u = __float_as_uint(v);
  auto r = __builtin_amdgcn_permlane32_swap(u, u, false, false);
  float a = fmaxf(__uint_as_float(r[0]), __uint_as_float(r[1]));
  u = __float_as_uint(a);
  auto s = __builtin_amdgcn_permlane16_swap(u, u, false, false);
  return fmaxf(__uint_as_float(s[0]), __uint_as_float(s[1]));
}
DI float xsum(float v) {
  unsigned u = __float_as_uint(v);
  auto r = __builtin_amdgcn_permlane32_swap(u, u, false, false);
  float a = __uint_as_float(r[0]) + __uint_as_float(r[1]);
  u = __float_as_uint(a);
  auto s = __builtin_amdgcn_permlane16_swap(u, u, false, false);
  return __uint_as_float(s[0]) + __uint_as_float(s[1]);
}
DI u32x4 pack_nat(f32x4 t0, f32x4 t1, float sc) {
  unsigned a0 = cvtpk(t0[0] * sc, t0[1] * sc), a1 = cvtpk(t0[2] * sc, t0[3] * sc);
  unsigned b0 = cvtpk(t1[0] * sc, t1[1] * sc), b1 = cvtpk(t1[2] * sc, t1[3] * sc);
  auto r0 = __builtin_amdgcn_permlane32_swap(a0, b0, false, false);
  auto r1 = __builtin_amdgcn_permlane32_swap(a1, b1, false, false);
  auto s0 = __builtin_amdgcn_permlane16_swap(r0[0], r0[1], false, false);
  auto s1 = __builtin_amdgcn_permlane16_swap(r1[0], r1[1], false, false);
  return u32x4{s0[0], s1[0], s0[1], s1[1]};
}
DI u32x4 pack_acc(f32x4 t0, f32x4 t1, float sc) {
  return u32x4{cvtpk(t0[0] * sc, t0[1] * sc), cvtpk(t0[2] * sc, t0[3] * sc), cvtpk(t1[0] * sc, t1[1] * sc), cvtpk(t1[2] * sc, t1[3] * sc)};
}
DI unsigned scale_pk(unsigned p, float a) {
  return cvtpk(__uint_as_float(p << 16) * a, __uint_as_float(p & 0xffff0000u) * a);
}
DI int imax3(int a, int b, int c) { return max(max(a, b), c); }
DI int imax8(const f32x4& a, const f32x4& b, int m) {
  m = imax3(m, __float_as_int(a[0]), __float_as_int(a[1]));
  m = imax3(m, __float_as_int(a[2]), __float_as_int(a[3]));
  m = imax3(m, __float_as_int(b[0]), __float_as_int(b[1]));
  m = imax3(m, __float_as_int(b[2]), __float_as_int(b[3]));
  return m;
}
DI float fmax8(const f32x4& a, const f32x4& b) {
  return fmaxf(fmaxf(fmaxf(a[0], a[1]), fmaxf(a[2], a[3])), fmaxf(fmaxf(b[0], b[1]), fmaxf(b[2], b[3])));
}

#define STAMP()
#define SB() __builtin_amdgcn_sched_barrier(0)

DI u32x4 make_rsrc(const void* p, unsigned bytes) {
  const unsigned long long v = (unsigned long long)p;
  return u32x4{(unsigned)v, (unsigned)(v >> 32) & 0xffffu, bytes, 0x00020000u};
}
template <int I> DI void dma1(unsigned mbase, unsigned voff, u32x4 r, unsigned soff) {
  constexpr int MO = (I >> 1) * 8192 + (I & 1) * (4096 - 128), IO = (I & 1) * 128;
  asm volatile("s_add_u32 m0, %0, %4\n\ts_nop 0\n\tbuffer_load_dwordx4 %1, %2, %3 offen offset:%5 nt lds"
               :: "s"(mbase), "v"(voff), "s"(r), "s"(soff), "i"(MO), "i"(IO) : "memory", "scc");
}
template <unsigned MO, unsigned SO, bool NT> DI void dmaq(unsigned mbase, unsigned voff, u32x4 r, unsigned sbase) {
  unsigned t;
  if constexpr (NT)
    asm volatile("s_add_u32 m0, %1, %5\n\ts_add_u32 %0, %4, %6\n\tbuffer_load_dwordx4 %2, %3, %0 offen nt lds"
                 : "=&s"(t) : "s"(mbase), "v"(voff), "s"(r), "s"(sbase), "i"(MO), "i"(SO) : "memory", "scc");
  else
    asm volatile("s_add_u32 m0, %1, %5\n\ts_add_u32 %0, %4, %6\n\tbuffer_load_dwordx4 %2, %3, %0 offen lds"
                 : "=&s"(t) : "s"(mbase), "v"(voff), "s"(r), "s"(sbase), "i"(MO), "i"(SO) : "memory", "scc");
}
template <unsigned MO, unsigned SO> DI void dmah(unsigned mbase, unsigned voff, u32x4 r) {
  unsigned t;
  asm volatile("s_add_u32 m0, %1, %4\n\ts_mov_b32 %0, %5\n\ts_mov_b32 exec_hi, 0\n\tbuffer_load_dwordx4 %2, %3, %0 offen lds\n\ts_mov_b32 exec_hi, -1"
               : "=&s"(t) : "s"(mbase), "v"(voff), "s"(r), "i"(MO), "i"(SO) : "memory", "scc");
}
constexpr int WSL[10] = {0, 1, 2, 3, 0, 1, 3, 0, 1, 3}, XSL[10] = {2, 3, 0, 1, 2, 2, 2, 2, 2, 2};
struct XS { u32x4 r; unsigned ve, vo, v9, mw; };
template <int N, int I> DI void dma_x(const XS& x) { dmaq<XSL[N] * 16384 + I * 4096, N * 128 + (I >> 1) * 16 * 1280, false>(x.mw, (I & 1) ? x.vo : x.ve, x.r, 0u); }
template <int N> DI void dma_x9(const XS& x) { dmah<L_XN + (N % 3) * 2048, N * 128>(x.mw >> 1, x.v9, x.r); }
template <int N> DI void sync_n() { SB(); asm volatile("s_waitcnt vmcnt(%0) lgkmcnt(0)\n\ts_barrier" :: "i"(N) : "memory"); SB(); }
DI void sync_lds() { SB(); asm volatile("s_waitcnt lgkmcnt(0)\n\ts_barrier" ::: "memory"); SB(); }

__shared__ __attribute__((aligned(1024))) char smem1[L_END];
DI u32x4 lds128(unsigned off) { return *(const LDSP u32x4*)((LDSP char*)smem1 + off); }
DI f32x4 lds128f(unsigned off) { return *(const LDSP f32x4*)((LDSP char*)smem1 + off); }
DI f32x2 lds64f(unsigned off) { return *(const LDSP f32x2*)((LDSP char*)smem1 + off); }
DI void sts128(unsigned off, u32x4 v) { *(LDSP u32x4*)((LDSP char*)smem1 + off) = v; }
DI void sts64(unsigned off, u32x2 v) { *(LDSP u32x2*)((LDSP char*)smem1 + off) = v; }
DI void sts64f(unsigned off, f32x2 v) { *(LDSP f32x2*)((LDSP char*)smem1 + off) = v; }
DI void sts32(unsigned off, float v) { *(LDSP float*)((LDSP char*)smem1 + off) = v; }
DI u32x4 ldstr(unsigned off, unsigned off2) {
  u32x2 a = __builtin_bit_cast(u32x2, __builtin_amdgcn_ds_read_tr16_b64_v4i16((LDSP s16x4*)((LDSP char*)smem1 + off)));
  u32x2 b = __builtin_bit_cast(u32x2, __builtin_amdgcn_ds_read_tr16_b64_v4i16((LDSP s16x4*)((LDSP char*)smem1 + off2)));
  return u32x4{a[0], a[1], b[0], b[1]};
}
#define MFS "v_mfma_f32_16x16x32_bf16 "
#define M_(D, A, B) MFS "%[" #D "], %[" #A "], %[" #B "], %[" #D "]\n\t"
#define MC(D, A, B, C) MFS "%[" #D "], %[" #A "], %[" #B "], %[" #C "]\n\t"
#define E_(D, S) "v_exp_f32 %[" #D "], %[" #S "]\n\t"
#define A_(L, P) "v_add_f32 %[" #L "], %[" #L "], %[" #P "]\n\t"
#define C_(D, X, Y) "v_cvt_pk_bf16_f32 %[" #D "], %[" #X "], %[" #Y "]\n\t"
#define X_(M, X, Y) "v_max3_f32 %[" #M "], %[" #M "], %[" #X "], %[" #Y "]\n\t"
DI void mf(f32x4& d, u32x4 a, u32x4 b) { asm volatile(M_(d, a, b) : [d] "+v"(d) : [a] "v"(a), [b] "v"(b)); }
DI void mfc(f32x4& d, u32x4 a, u32x4 b, f32x4 c) { asm volatile(MC(d, a, b, c) : [d] "=&v"(d) : [a] "v"(a), [b] "v"(b), [c] "v"(c)); }
DI void mf2(f32x4& d, f32x4& e, u32x4 a, u32x4 b, u32x4 c) {
  asm volatile(M_(d, a, b) M_(e, a, c) : [d] "+v"(d), [e] "+v"(e) : [a] "v"(a), [b] "v"(b), [c] "v"(c));
}
DI void mf3(f32x4& d, f32x4& e, f32x4& f, u32x4 a, u32x4 b, u32x4 c, u32x4 g) {
  asm volatile(M_(d, a, b) M_(e, a, c) M_(f, a, g) : [d] "+v"(d), [e] "+v"(e), [f] "+v"(f) : [a] "v"(a), [b] "v"(b), [c] "v"(c), [g] "v"(g));
}
DI void mf2z(f32x4& d, f32x4& e, u32x4 a, u32x4 b, u32x4 c) {
  asm volatile(MFS "%0, %2, %3, 0\n\t" MFS "%1, %2, %4, 0" : "=&v"(d), "=&v"(e) : "v"(a), "v"(b), "v"(c));
}
DI void mf3z(f32x4& d, f32x4& e, f32x4& f, u32x4 a, u32x4 b, u32x4 c, u32x4 g) {
  asm volatile(MFS "%0, %3, %4, 0\n\t" MFS "%1, %3, %5, 0\n\t" MFS "%2, %3, %6, 0" : "=&v"(d), "=&v"(e), "=&v"(f) : "v"(a), "v"(b), "v"(c), "v"(g));
}
DI void mfma_fence() { asm volatile("s_nop 7\n\ts_nop 1" ::: "memory"); SB(); }

struct Att {
  f32x4 O[2][8], O9[2];
  u32x4 Q[2][4];
  f32x4 negm[2], negm9, S9;
  u32x4 VH[3], p9;
  float l[2], l9;
  unsigned kb[4], vb[8], l16, m9a;
  u32x4 rA, rB;
  unsigned vA, vB, sA, sB, inc, mb;
};
template <int I> DI void dma_s(Att& a) {
  if constexpr (I < 2) dma1<I>(a.mb, a.vA, a.rA, a.sA); else dma1<I>(a.mb, a.vB, a.rB, a.sB);
  if constexpr (I == 3) { a.sA += a.inc; a.sB += a.inc; a.mb = (a.mb + 16384u) & 0xffffu; }
}
DI void exp4(const f32x4& s, float (&p)[4]) {
#pragma unroll
  for (int j = 0; j < 4; j++) p[j] = ex2(s[j]);
}
DI float sum4(float l, const float (&p)[4]) { return (((l + p[0]) + p[1]) + p[2]) + p[3]; }

template <bool MAIN> DI void pin_state(Att& a, f32x4 (&S)[2][2], u32x4 (&PN)[2], u32x4 (&PO)[2]) {
  if constexpr (MAIN) asm volatile("" : "+v"(S[0][0]), "+v"(S[0][1]), "+v"(S[1][0]), "+v"(S[1][1]), "+v"(PN[0]), "+v"(PN[1]), "+v"(PO[0]), "+v"(PO[1]),
                                   "+v"(a.negm[0]), "+v"(a.negm[1]), "+v"(a.S9), "+v"(a.negm9), "+v"(a.p9));
  else asm volatile("" : "+v"(a.negm9), "+v"(a.p9));
}
template <bool MAIN, bool PEND9> DI void rescale(Att& a, f32x4 (&S)[2][2], u32x4 (&PN)[2], u32x4 (&PO)[2], f32x2 npr) {
  mfma_fence();
  const f32x2 pr = -npr;
  if constexpr (MAIN) {
#pragma unroll
    for (int qa = 0; qa < 2; qa++) {
      const float d = fmaxf(xmax(fmax8(S[qa][0], S[qa][1])), 0.f);
      const float al = ex2(-d);
      S[qa][0] -= d; S[qa][1] -= d; a.negm[qa] -= d; a.l[qa] *= al;
#pragma unroll
      for (int i = 0; i < 8; i++) a.O[qa][i] *= al;
#pragma unroll
      for (int j = 0; j < 4; j++) { PN[qa][j] = scale_pk(PN[qa][j], al); PO[qa][j] = scale_pk(PO[qa][j], al); }
    }
  }
  const float r = -a.negm9[0];
  float rn = fmaxf(r, fmaxf(pr[0], pr[1]));
  if constexpr (PEND9) rn = fmaxf(rn, r + xmax(fmaxf(fmaxf(a.S9[0], a.S9[1]), fmaxf(a.S9[2], a.S9[3]))));
  const float al = ex2(r - rn), fa = ex2(pr[0] - rn), fb = ex2(pr[1] - rn);
  a.O9[0] *= al; a.O9[1] *= al; a.l9 *= al;
  if constexpr (PEND9) a.S9 -= rn - r;
  a.p9 = u32x4{scale_pk(a.p9[0], fa), scale_pk(a.p9[1], fa), scale_pk(a.p9[2], fb), scale_pk(a.p9[3], fb)};
  a.negm9 = f32x4{-rn, -rn, -rn, -rn};
  pin_state<MAIN>(a, S, PN, PO);
}
template <int W, int SLOT> DI void sm9(Att& a) {
  float p[4];
  exp4(a.S9, p);
  a.l9 = sum4(a.l9, p);
  sts64(a.l16 + (L_P9 + SLOT * 1024 + (W & 1) * 8), u32x2{cvtpk(p[0], p[1]), cvtpk(p[2], p[3])});
  sts32(a.m9a + (SLOT * 128 + (W & 1) * 4), a.negm9[0]);
}
#define PV2(I, VF, P) mf2(a.O[0][I], a.O[1][I], VF, P[0], P[1])
#define PV3(I, VF, P) mf3(a.O[0][I], a.O[1][I], a.O9[I], VF, P[0], P[1], a.p9)
#define RDV(I) ldstr(a.vb[I] + OV, a.vb[I] + OV + 2048)
#define RDK(KT, S) lds128(a.kb[S] + OK + (KT) * 2048)
#define RDQ9(S) lds128(a.l16 + (L_Q9 + ((((S) + W) & 3) << 10)))
#define QK_OUT [d] "+v"(SN[0][0]), [e] "+v"(SN[1][0]), [f] "+v"(SN[0][1]), [g] "+v"(SN[1][1])
#define QK_IN(S) [k0] "v"(K[0][S]), [k1] "v"(K[1][S]), [q0] "v"(a.Q[0][S]), [q1] "v"(a.Q[1][S])
#define PV_OUT(I, J) [d] "+v"(a.O[0][I]), [e] "+v"(a.O[1][I]), [f] "+v"(a.O[0][J]), [g] "+v"(a.O[1][J])
#define PV_IN(V0, V1) [k0] "v"(V0), [k1] "v"(V1), [q0] "v"(PO[0]), [q1] "v"(PO[1])
#define P4(N, P) [N##0] "v"(P[0]), [N##1] "v"(P[1]), [N##2] "v"(P[2]), [N##3] "v"(P[3])
#define BLK4(F0, F1, F2, F3) M_(d, k0, q0) F0 M_(e, k0, q1) F1 M_(f, k1, q0) F2 M_(g, k1, q1) F3
template <int W, bool ODD, bool LAST> DI void att_step(Att& a, const XS& xs, f32x4 (&SO)[2][2], f32x4 (&SN)[2][2], u32x4 (&PO)[2], u32x4 (&PN)[2]) {
  constexpr unsigned OV = ODD ? 16384u : 0u, OK = OV + 8192u;
  constexpr bool Q9 = ((W >> 1) == (ODD ? 0 : 1)) && !LAST, M9 = (W >> 1) == (ODD ? 1 : 0);
  constexpr int KT9 = W & 1;
  u32x4 K[2][4], QF[4], V[5];
  float p0[4], p1[4], p2[4], p3[4];
  unsigned c0, c1, c2, c3;
  sync_n<8>();
  STAMP();
  if constexpr (!LAST) {
    K[0][0] = RDK(0, 0); K[1][0] = RDK(1, 0);
    if constexpr (Q9) QF[0] = RDQ9(0);
    K[0][1] = RDK(0, 1); K[1][1] = RDK(1, 1);
  } else { V[0] = RDV(2); V[1] = RDV(3); }
  SB();
  asm volatile(M_(d, v, q0) E_(x, sx) M_(e, v, q1) E_(y, sy)
               : [d] "+v"(a.O[0][7]), [e] "+v"(a.O[1][7]), [x] "=&v"(p0[0]), [y] "=&v"(p0[1])
               : [v] "v"(a.VH[0]), [q0] "v"(PN[0]), [q1] "v"(PN[1]), [sx] "v"(SO[0][0][0]), [sy] "v"(SO[0][0][1]));
  if constexpr (LAST) dma_x<0, 0>(xs); else dma_s<0>(a);
  if constexpr (Q9) { QF[1] = RDQ9(1); SB(); }
  asm volatile(M_(d, v, q0) E_(x, sx) M_(e, v, q1) E_(y, sy) M_(f, v, q2) E_(z, sz)
               : [d] "+v"(a.O[0][0]), [e] "+v"(a.O[1][0]), [f] "+v"(a.O9[0]), [x] "=&v"(p0[2]), [y] "=&v"(p0[3]), [z] "=&v"(p1[0])
               : [v] "v"(a.VH[1]), [q0] "v"(PN[0]), [q1] "v"(PN[1]), [q2] "v"(a.p9), [sx] "v"(SO[0][0][2]), [sy] "v"(SO[0][0][3]), [sz] "v"(SO[0][1][0]));
  SB();
  if constexpr (!LAST) { K[0][2] = RDK(0, 2); K[1][2] = RDK(1, 2); if constexpr (Q9) QF[2] = RDQ9(2); }
  else { V[2] = RDV(4); V[3] = RDV(5); }
  SB();
  asm volatile(M_(d, v, q0) E_(x, sx) M_(e, v, q1) E_(y, sy) M_(f, v, q2) E_(z, sz)
               : [d] "+v"(a.O[0][1]), [e] "+v"(a.O[1][1]), [f] "+v"(a.O9[1]), [x] "=&v"(p1[1]), [y] "=&v"(p1[2]), [z] "=&v"(p1[3])
               : [v] "v"(a.VH[2]), [q0] "v"(PN[0]), [q1] "v"(PN[1]), [q2] "v"(a.p9), [sx] "v"(SO[0][1][1]), [sy] "v"(SO[0][1][2]), [sz] "v"(SO[0][1][3]));
  SB();
  if constexpr (!LAST) {
    asm volatile(MC(d, k0, q0, n0) C_(c0, x0, x1) C_(c1, x2, x3) MC(e, k0, q1, n1) C_(c2, y0, y1) C_(c3, y2, y3)
                 MC(f, k1, q0, n0) A_(l, x0) A_(l, x1) MC(g, k1, q1, n1) A_(l, x2) A_(l, x3)
                 : [d] "=&v"(SN[0][0]), [e] "=&v"(SN[1][0]), [f] "=&v"(SN[0][1]), [g] "=&v"(SN[1][1]),
                   [c0] "=&v"(c0), [c1] "=&v"(c1), [c2] "=&v"(c2), [c3] "=&v"(c3), [l] "+v"(a.l[0])
                 : QK_IN(0), [n0] "v"(a.negm[0]), [n1] "v"(a.negm[1]), P4(x, p0), P4(y, p1));
    PN[0] = u32x4{c0, c1, c2, c3};
    if constexpr (Q9) mfc(a.S9, K[KT9][0], QF[0], a.negm9);
    SB();
    K[0][3] = RDK(0, 3); K[1][3] = RDK(1, 3); if constexpr (Q9) QF[3] = RDQ9(3);
    SB();
    dma_s<1>(a);
    asm volatile(BLK4(A_(l, y0) A_(l, y1), A_(l, y2) A_(l, y3), E_(x, sx), E_(y, sy))
                 : QK_OUT, [l] "+v"(a.l[0]), [x] "=&v"(p2[0]), [y] "=&v"(p2[1])
                 : QK_IN(1), P4(y, p1), [sx] "v"(SO[1][0][0]), [sy] "v"(SO[1][0][1]));
    if constexpr (Q9) mf(a.S9, K[KT9][1], QF[1]);
    SB();
    V[0] = RDV(2); V[1] = RDV(3);
    SB();
    asm volatile(BLK4(E_(x, sx), E_(y, sy), E_(z, sz), E_(u, su))
                 : QK_OUT, [x] "=&v"(p2[2]), [y] "=&v"(p2[3]), [z] "=&v"(p3[0]), [u] "=&v"(p3[1])
                 : QK_IN(2), [sx] "v"(SO[1][0][2]), [sy] "v"(SO[1][0][3]), [sz] "v"(SO[1][1][0]), [su] "v"(SO[1][1][1]));
    if constexpr (Q9) mf(a.S9, K[KT9][2], QF[2]);
    SB();
    V[2] = RDV(4); V[3] = RDV(5);
    SB();
    dma_s<2>(a);
    asm volatile(BLK4(E_(x, sx), E_(y, sy), A_(l, y0) A_(l, y1), A_(l, y2) A_(l, y3))
                 : QK_OUT, [l] "+v"(a.l[1]), [x] "=&v"(p3[2]), [y] "=&v"(p3[3])
                 : QK_IN(3), P4(y, p2), [sx] "v"(SO[1][1][2]), [sy] "v"(SO[1][1][3]));
    if constexpr (Q9) mf(a.S9, K[KT9][3], QF[3]);
    SB();
  } else {
    PN[0] = u32x4{cvtpk(p0[0], p0[1]), cvtpk(p0[2], p0[3]), cvtpk(p1[0], p1[1]), cvtpk(p1[2], p1[3])};
    a.l[0] = sum4(sum4(a.l[0], p0), p1);
    exp4(SO[1][0], p2); exp4(SO[1][1], p3);
    a.l[1] = sum4(a.l[1], p2);
    asm volatile("" : "+v"(PN[0]), "+v"(a.l[0]), "+v"(a.l[1]), "+v"(p3[0]), "+v"(p3[1]), "+v"(p3[2]), "+v"(p3[3]));
    dma_x<0, 1>(xs); dma_x<0, 2>(xs);
    SB();
  }
  V[4] = RDV(6);
  a.p9 = lds128(a.l16 + (L_P9 + (ODD ? 0 : 1024)));
  const f32x2 npr = lds64f(a.m9a + (ODD ? 0 : 128));
  a.VH[0] = RDV(7);
  SB();
  asm volatile(BLK4(C_(c0, x0, x1) C_(c1, x2, x3), C_(c2, y0, y1) C_(c3, y2, y3), A_(l, y0) A_(l, y1), A_(l, y2) A_(l, y3))
               : PV_OUT(2, 3), [c0] "=&v"(c0), [c1] "=&v"(c1), [c2] "=&v"(c2), [c3] "=&v"(c3), [l] "+v"(a.l[1])
               : PV_IN(V[0], V[1]), P4(x, p2), P4(y, p3));
  PN[1] = u32x4{c0, c1, c2, c3};
  SB();
  a.VH[1] = RDV(0); a.VH[2] = RDV(1);
  SB();
  if constexpr (LAST) dma_x<0, 3>(xs); else dma_s<3>(a);
  float mx = 0.f;
  if constexpr (M9) {
    asm volatile(BLK4(E_(x0, s0) E_(x1, s1), E_(x2, s2) E_(x3, s3), C_(c0, x0, x1) A_(l, x0) A_(l, x1), C_(c1, x2, x3) A_(l, x2) A_(l, x3))
                 : PV_OUT(4, 5), [x0] "=&v"(p0[0]), [x1] "=&v"(p0[1]), [x2] "=&v"(p0[2]), [x3] "=&v"(p0[3]), [c0] "=&v"(c0), [c1] "=&v"(c1), [l] "+v"(a.l9)
                 : PV_IN(V[2], V[3]), P4(s, a.S9));
    sts64(a.l16 + (L_P9 + (ODD ? 1024 : 0) + KT9 * 8), u32x2{c0, c1});
    sts32(a.m9a + ((ODD ? 128 : 0) + KT9 * 4), a.negm9[0]);
    SB();
    if constexpr (!LAST)
      asm volatile(M_(d, v, q0) X_(m, x0, x1) X_(m, x2, x3) X_(m, y0, y1) X_(m, y2, y3) M_(e, v, q1) X_(m, z0, z1) X_(m, z2, z3) X_(m, u0, u1) X_(m, u2, u3)
                   : [d] "+v"(a.O[0][6]), [e] "+v"(a.O[1][6]), [m] "+v"(mx)
                   : [v] "v"(V[4]), [q0] "v"(PO[0]), [q1] "v"(PO[1]), P4(x, SN[0][0]), P4(y, SN[1][0]), P4(z, SN[0][1]), P4(u, SN[1][1]));
    else PV2(6, V[4], PO);
  } else if constexpr (!LAST) {
    asm volatile(BLK4(X_(m, x0, x1) X_(m, x2, x3), X_(m, y0, y1) X_(m, y2, y3), X_(m, z0, z1) X_(m, z2, z3), X_(m, u0, u1) X_(m, u2, u3))
                 : PV_OUT(4, 5), [m] "+v"(mx)
                 : PV_IN(V[2], V[3]), P4(x, SN[0][0]), P4(y, SN[1][0]), P4(z, SN[0][1]), P4(u, SN[1][1]));
    asm volatile(M_(d, v, q0) X_(m, x0, x1) X_(m, x2, x3) M_(e, v, q1)
                 : [d] "+v"(a.O[0][6]), [e] "+v"(a.O[1][6]), [m] "+v"(mx)
                 : [v] "v"(V[4]), [q0] "v"(PO[0]), [q1] "v"(PO[1]), P4(x, a.S9));
  } else { PV2(4, V[2], PO); PV2(5, V[3], PO); PV2(6, V[4], PO); }
  if constexpr (LAST) sts64f(a.m9a * 4 + (L_L9 - 4 * L_M9 + W * 8), f32x2{xsum(a.l9), a.negm9[0]});
  if constexpr (ODD && !LAST) {
#pragma unroll
    for (int s = 0; s < 4; s++) a.kb[s] ^= 0x8000u;
#pragma unroll
    for (int i = 0; i < 8; i++) a.vb[i] ^= 0x8000u;
  }
  const bool rare = (mx > THR) | (npr[0] != a.negm9[0]) | (npr[1] != a.negm9[0]);
  pin_state<!LAST>(a, SN, PN, PO);
  if (__builtin_amdgcn_ballot_w64(rare) != 0) rescale<!LAST, Q9>(a, SN, PN, PO, npr);
  pin_state<!LAST>(a, SN, PN, PO);
}

struct Nxt { u32x4 rV, rN; unsigned voV, voN, sV15, sN, dN, incN; };
template <int W> DI void attention(Att& a, const Nxt& nx, const XS& xs, u32x4 (&PA)[2], u32x4 (&PB)[2]) {
  f32x4 SA[2][2], SB_[2][2];
  const f32x4 z = {0.f, 0.f, 0.f, 0.f};
  {
    sync_n<8>();
    STAMP();
    u32x4 K[2][4], QF[4];
    constexpr int T9 = W >> 1, KT9 = W & 1;
#pragma unroll
    for (int tt = 0; tt < 2; tt++) {
      const int tl = tt ^ T9;
      const unsigned OK = tl * 8192;
      f32x4 (&S)[2][2] = tl == 0 ? SA : SB_;
#pragma unroll
      for (int s = 0; s < 4; s++) { K[0][s] = RDK(0, s); K[1][s] = RDK(1, s); if (tt == 0) QF[s] = RDQ9(s); }
      if (tt == 0) { dma_s<0>(a); dma_s<1>(a); } else { dma_s<2>(a); dma_s<3>(a); }
#pragma unroll
      for (int s = 0; s < 4; s++) {
        if (tt == 0) a.S9 = mfma(K[KT9][s], QF[s], s == 0 ? z : a.S9);
#pragma unroll
        for (int kt = 0; kt < 2; kt++) {
          S[0][kt] = mfma(K[kt][s], a.Q[0][s], s == 0 ? z : S[0][kt]);
          S[1][kt] = mfma(K[kt][s], a.Q[1][s], s == 0 ? z : S[1][kt]);
        }
      }
      if (tt == 1) sts32(a.m9a * 2 + (L_X9 - 2 * L_M9 + W * 4), xmax(fmaxf(fmaxf(a.S9[0], a.S9[1]), fmaxf(a.S9[2], a.S9[3]))));
    }
    sync_lds();
    const f32x4 m4 = lds128f(a.m9a * 2 + (L_X9 - 2 * L_M9));
#pragma unroll
    for (int qa = 0; qa < 2; qa++) {
      const float m = xmax(fmaxf(fmax8(SA[qa][0], SA[qa][1]), fmax8(SB_[qa][0], SB_[qa][1])));
      SA[qa][0] -= m; SA[qa][1] -= m; SB_[qa][0] -= m; SB_[qa][1] -= m;
      a.negm[qa] = f32x4{-m, -m, -m, -m};
      float p0[4], p1[4];
      exp4(SA[qa][0], p0); exp4(SA[qa][1], p1);
      a.l[qa] = sum4(sum4(0.f, p0), p1);
      PA[qa] = u32x4{cvtpk(p0[0], p0[1]), cvtpk(p0[2], p0[3]), cvtpk(p1[0], p1[1]), cvtpk(p1[2], p1[3])};
      PB[qa] = u32x4{0u, 0u, 0u, 0u};
    }
    const float m9 = fmaxf(fmaxf(m4[0], m4[1]), fmaxf(m4[2], m4[3]));
    a.S9 -= m9; a.negm9 = f32x4{-m9, -m9, -m9, -m9}; a.l9 = 0.f;
    if constexpr (T9 == 0) sm9<W, 0>(a);
#pragma unroll
    for (int i = 0; i < 8; i++) { a.O[0][i] = z; a.O[1][i] = z; }
    a.O9[0] = z; a.O9[1] = z;
    a.VH[0] = PB[0]; a.VH[1] = PB[0]; a.VH[2] = PB[0]; a.p9 = PB[0];
  }
#pragma unroll 1
  for (int t = 1; t < 15; t += 2) {
    att_step<W, true, false>(a, xs, SB_, SA, PA, PB);
    if (t == 11) { a.rB = nx.rV; a.vB = nx.voV; a.sB = nx.sV15; }
    att_step<W, false, false>(a, xs, SA, SB_, PB, PA);
    if (t == 11) { a.rA = nx.rN; a.rB = nx.rN; a.vA = nx.voN; a.vB = nx.voN; a.sA = nx.sN; a.sB = nx.sN + nx.dN; a.inc = nx.incN; }
  }
  att_step<W, true, true>(a, xs, SB_, SA, PA, PB);
}
DI void drain(Att& a, const XS& xs, u32x4 (&PA)[2], u32x4 (&PB)[2], float& l9out) {
  f32x4 SA[2][2];
  constexpr unsigned OV = 16384u + 8192u;
  sync_lds();
  STAMP();
  u32x4 V[8];
  V[2] = RDV(2); V[3] = RDV(3);
  const u32x4 p9n = lds128(a.l16 + (L_P9 + 1024));
  const f32x2 pr = lds64f(a.m9a + 128);
  V[4] = RDV(4); V[5] = RDV(5);
  SB();
  PV2(7, a.VH[0], PA); dma_x9<0>(xs);
  PV3(0, a.VH[1], PA);
  SB();
  V[6] = RDV(6); V[7] = RDV(7);
  SB();
  PV3(1, a.VH[2], PA); dma_x9<1>(xs);
  SB();
  V[0] = RDV(0); V[1] = RDV(1);
  const f32x4 la = lds128f(a.m9a * 4 + (L_L9 - 4 * L_M9)), lb = lds128f(a.m9a * 4 + (L_L9 - 4 * L_M9 + 16));
  SB();
  PV2(2, V[2], PB); PV2(3, V[3], PB); PV2(4, V[4], PB); PV2(5, V[5], PB);
  PV2(6, V[6], PB); PV2(7, V[7], PB); PV2(0, V[0], PB); PV2(1, V[1], PB);
  a.p9 = p9n;
  if (__builtin_amdgcn_ballot_w64((pr[0] != a.negm9[0]) | (pr[1] != a.negm9[0])) != 0) rescale<false, false>(a, SA, PA, PB, pr);
  asm volatile("s_nop 1" : "+v"(a.p9), "+v"(a.O9[0]), "+v"(a.O9[1]));
  mf(a.O9[0], V[0], a.p9); mf(a.O9[1], V[1], a.p9);
  mfma_fence();
  const float n9 = a.negm9[0];
  l9out = (la[0] * ex2(n9 - la[1]) + la[2] * ex2(n9 - la[3])) + (lb[0] * ex2(n9 - lb[1]) + lb[2] * ex2(n9 - lb[3]));
}

__global__ __launch_bounds__(256, 2) void attn_kernel(const u16* __restrict__ x, const u16* __restrict__ kc, const u16* __restrict__ vc,
                                                      const u16* __restrict__ wq, const u16* __restrict__ wo, char* __restrict__ ws) {
  const int w = __builtin_amdgcn_readfirstlane(threadIdx.x >> 6);
  const int bid = blockIdx.x, xcd = bid & 7, idx = bid >> 3;
  const int b = xcd * 4 + (idx & 3), hp = idx >> 2;
#define XKV 1
#define XX 1
#define XW 1
  const u32x4 rK = make_rsrc(kc, XKV * NB * NH * SK * DH * 2), rV = make_rsrc(vc, XKV * NB * NH * SK * DH * 2);
  const u32x4 rW = make_rsrc(wq, XW * DM * NH * DH * 2), rX = make_rsrc(x, XX * NB * SQ * DM * 2), rWo = make_rsrc(wo, DM * NH * DH * 2);
  const __amdgpu_buffer_rsrc_t rO = __builtin_amdgcn_make_buffer_rsrc((void*)(ws + WS_O), (short)0, NB * 128 * 9 * 1024, 0x00020000);

  unsigned lane = threadIdx.x & 63;
  Att a;
  STAMP();
  const unsigned mw = w * 1024;
#define DMA_OFFS()                                                                                                   \
  const unsigned drow = 8 * w + (lane >> 3), dch = lane & 7;                                                         \
  const unsigned voW = drow * 8192 + ((dch ^ ((((drow >> 1) & 1) | (((drow >> 3) & 1) << 1)) << 1)) << 4);           \
  XS xs;                                                                                                             \
  xs.r = rX; xs.mw = mw;                                                                                             \
  xs.ve = (b * SQ + 32 * w + (lane >> 3)) * 1280 + ((dch ^ (lane >> 4)) << 4);                                       \
  xs.vo = (xs.ve ^ 64u) + 8 * 1280;                                                                                  \
  xs.v9 = (b * SQ + 128 + 4 * w + ((lane >> 3) & 3)) * 1280 + ((dch ^ (2 * w + ((lane >> 4) & 1))) << 4);
#define DMA_W(N, J) dmaq<WSL[N] * 16384 + ((J) >> 1) * 8192 + ((J) & 1) * 4096, ((N) * 64 + ((J) >> 1) * 32) * 8192 + ((J) & 1) * 128, false>(mw, voW, rW, sw)
  {
    DMA_OFFS();
    const unsigned sw = (unsigned)hp * 256u;
    dma_x<0, 0>(xs); dma_x<0, 1>(xs); dma_x<0, 2>(xs); dma_x<0, 3>(xs); dma_x9<0>(xs);
    DMA_W(0, 0); DMA_W(0, 1); DMA_W(0, 2); DMA_W(0, 3);
    dma_x9<1>(xs);
    DMA_W(1, 0); DMA_W(1, 1); DMA_W(1, 2); DMA_W(1, 3);
    asm volatile("s_waitcnt vmcnt(5)" ::: "memory");
  }

#pragma unroll 1
  for (int hi = 0; hi < 2; hi++) {
    const int h = hp + 16 * hi;
    const unsigned sw = (unsigned)h * 256u;
    const unsigned skv = (unsigned)(b * NH + h) * (SK * DH * 2);
    asm volatile("" : "+v"(lane));
    DMA_OFFS();
    const unsigned voK = drow * 256 + ((dch ^ ((drow >> 1) & 7)) << 4);
    const unsigned voV = drow * 256 + ((dch ^ (((drow >> 1) & 3) << 1)) << 4);
    const unsigned li = lane & 15, lg = lane >> 4, lq = (lane >> 2) & 3, lp = lane & 3;
    {
      unsigned wb[8];
      const unsigned row = 8 * lg + lq, fw = (((row >> 1) & 1) | (((row >> 3) & 1) << 1)) << 1;
#pragma unroll
      for (int i = 0; i < 8; i++) {
        const unsigned dt = (i + 2 * w) & 7;
        wb[i] = (dt >> 2) * 4096 + row * 128 + (((2 * (dt & 3) + (lp >> 1)) ^ fw) << 4) + 8 * (lp & 1);
      }
      const unsigned xsw = (lg ^ (li >> 1)) << 4;
      const unsigned xb0 = mw + (li >> 3) * 4096 + (li & 7) * 128 + xsw, xb1 = xb0 ^ 64u;
      const unsigned nb0 = L_XN + li * 128 + xsw, nb1 = nb0 ^ 64u;
      f32x4 acc[2][8], acc9[2];
      u32x4 WH[3], XH[3];
      auto qp_step = [&](auto nc) {
        constexpr int n = decltype(nc)::value;
        constexpr bool SWAP = n < 4;
        if (n == 0) sync_n<10>(); else if (n < 9) sync_n<5>(); else sync_n<4>();
        STAMP();
        auto rd = [&](int ks, int i) { const unsigned o = wb[i] + WSL[n] * 16384 + ks * 8192; return ldstr(o, o + 512); };
        u32x4 X0[3], X1[3];
        auto mm = [&](int i, u32x4 wf, const u32x4 (&f)[3], bool first = false) {
          if (first) { if (i < 2) mf3z(acc[0][i], acc[1][i], acc9[i], wf, f[0], f[1], f[2]); else mf2z(acc[0][i], acc[1][i], wf, f[0], f[1]); }
          else { if (i < 2) mf3(acc[0][i], acc[1][i], acc9[i], wf, f[0], f[1], f[2]); else mf2(acc[0][i], acc[1][i], wf, f[0], f[1]); }
        };
        auto d1 = [&](auto jc) {
          constexpr int j = decltype(jc)::value;
          if constexpr (n < 9) dma_x<n + 1, j>(xs);
          else dmaq<16384 + (j >> 1) * 8192 + (j & 1) * 4096, (j < 2 ? 0 : 2 * 8192) + (j & 1) * 128, true>(mw, j < 2 ? voV : voK, j < 2 ? rV : rK, skv);
        };
        auto d2 = [&](auto jc) {
          constexpr int j = decltype(jc)::value;
          if constexpr (n < 8) DMA_W(n + 2, j);
          else if constexpr (n == 8) dmaq<(j >> 1) * 8192 + (j & 1) * 4096, (j >> 1) * 8192 + (j & 1) * 128, true>(mw, voK, rK, skv);
          else dmaq<32768 + (j >> 1) * 8192 + (j & 1) * 4096, (j < 2 ? 8192 : 3 * 8192) + (j & 1) * 128, true>(mw, j < 2 ? voV : voK, j < 2 ? rV : rK, skv);
        };
        u32x4 F[8], G[5];
        X0[0] = lds128(xb0 + XSL[n] * 16384); X0[1] = lds128(xb0 + XSL[n] * 16384 + 8192);
        X1[0] = lds128(xb1 + XSL[n] * 16384); X1[1] = lds128(xb1 + XSL[n] * 16384 + 8192);
        X0[2] = lds128(nb0 + (n % 3) * 2048);
        F[0] = rd(0, 0); F[1] = rd(0, 1);
        SB();
        if constexpr (n > 0) mm(7, WH[0], XH);
        if constexpr (SWAP || n == 9) d1(IC<0>{});
        SB();
        F[2] = rd(0, 2); F[3] = rd(0, 3);
        SB();
        if constexpr (n > 0) mm(0, WH[1], XH);
        if constexpr (SWAP || n == 9) d1(IC<1>{});
        SB();
        F[4] = rd(0, 4); F[5] = rd(0, 5);
        SB();
        if constexpr (n > 0) mm(1, WH[2], XH);
        asm volatile("" :: "v"(X0[0]), "v"(X0[1]), "v"(X1[0]), "v"(X1[1]) : "memory");
        if constexpr (SWAP || n == 9) d1(IC<2>{}); else d1(IC<0>{});
        mm(0, F[0], X0, n == 0);
        if constexpr (SWAP || n == 9) d1(IC<3>{}); else d1(IC<1>{});
        SB();
        F[6] = rd(0, 6); F[7] = rd(0, 7); X1[2] = lds128(nb1 + (n % 3) * 2048);
        SB();
        mm(1, F[1], X0, n == 0);
        if constexpr (!(SWAP || n == 9)) d1(IC<2>{});
        mm(2, F[2], X0, n == 0);
        if constexpr (!(SWAP || n == 9)) d1(IC<3>{});
        SB();
        G[0] = rd(1, 2); G[1] = rd(1, 3);
        SB();
        mm(3, F[3], X0, n == 0); mm(4, F[4], X0, n == 0);
        if constexpr (n < 8) dma_x9<n + 2>(xs);
        SB();
        G[2] = rd(1, 4); G[3] = rd(1, 5);
        SB();
        mm(5, F[5], X0, n == 0); mm(6, F[6], X0, n == 0); d2(IC<0>{});
        SB();
        G[4] = rd(1, 6); WH[0] = rd(1, 7);
        SB();
        mm(7, F[7], X0, n == 0); mm(2, G[0], X1); d2(IC<1>{});
        SB();
        WH[1] = rd(1, 0); WH[2] = rd(1, 1);
        SB();
        mm(3, G[1], X1); mm(4, G[2], X1); d2(IC<2>{});
        mm(5, G[3], X1); d2(IC<3>{}); mm(6, G[4], X1);
        XH[0] = X1[0]; XH[1] = X1[1]; XH[2] = X1[2];
        if constexpr (n == 9) { mm(0, WH[1], XH); mm(1, WH[2], XH); mm(7, WH[0], XH); }
      };
      qp_step(IC<0>{}); qp_step(IC<1>{}); qp_step(IC<2>{}); qp_step(IC<3>{}); qp_step(IC<4>{});
      qp_step(IC<5>{}); qp_step(IC<6>{}); qp_step(IC<7>{}); qp_step(IC<8>{}); qp_step(IC<9>{});
      mfma_fence();
      sts128(L_Q9 + w * 1024 + lane * 16, pack_nat(acc9[0], acc9[1], QSCALE));
#pragma unroll
      for (int s = 0; s < 4; s++) {
        a.Q[0][s] = pack_nat(acc[0][2 * s], acc[0][2 * s + 1], QSCALE);
        a.Q[1][s] = pack_nat(acc[1][2 * s], acc[1][2 * s + 1], QSCALE);
      }
    }
#pragma unroll
    for (int s = 0; s < 4; s++) {
      const unsigned sa = (s + w) & 3;
      a.kb[s] = (sa >> 1) * 4096 + li * 128 + (((4 * (sa & 1) + lg) ^ (li >> 1)) << 4);
    }
    {
      const unsigned row = 4 * lg + lq, fv = ((row >> 1) & 3) << 1;
#pragma unroll
      for (int i = 0; i < 8; i++) {
        const unsigned dt = (i + 2 * w) & 7;
        a.vb[i] = (dt >> 2) * 4096 + row * 128 + (((2 * (dt & 3) + (lp >> 1)) ^ fv) << 4) + 8 * (lp & 1);
      }
    }
    a.l16 = lane * 16; a.m9a = L_M9 + li * 8;
    a.rA = rV; a.vA = voV; a.sA = skv + 2 * 8192; a.rB = rK; a.vB = voK; a.sB = skv + 4 * 8192; a.inc = 8192; a.mb = mw + 49152u;
    Nxt nx;
    nx.rV = rV; nx.voV = voV; nx.sV15 = skv + 15 * 8192;
    if (hi == 0) { nx.rN = rW; nx.voN = voW; nx.sN = sw + 16u * 256u; nx.dN = 32u * 8192u; nx.incN = 64u * 8192u; }
    else {
      nx.rN = rWo; nx.voN = voV + drow * (1280 - 256); nx.dN = 32u * 1280u; nx.incN = 0x20000000u;
      nx.sN = bid < 320 ? (unsigned)(bid / 5) * (64u * 1280u) + (unsigned)(bid % 5) * 256u : 0x20000000u;
    }
    float l9;
    u32x4 PA[2], PB[2];
    switch (w) {
      case 0: attention<0>(a, nx, xs, PA, PB); break;
      case 1: attention<1>(a, nx, xs, PA, PB); break;
      case 2: attention<2>(a, nx, xs, PA, PB); break;
      default: attention<3>(a, nx, xs, PA, PB); break;
    }
    drain(a, xs, PA, PB, l9);
    STAMP();
    {
      const unsigned so = (unsigned)(b * 128 + h * 4) * 9216u;
#pragma unroll
      for (int qa = 0; qa < 2; qa++) {
        const float inv = 1.0f / xsum(a.l[qa]);
#pragma unroll
        for (int u = 0; u < 4; u++)
          __builtin_amdgcn_raw_buffer_store_b128(pack_acc(a.O[qa][2 * u], a.O[qa][2 * u + 1], inv), rO,
                                                 a.l16 + so + ((u + w) & 3) * 9216 + (2 * w + qa) * 1024, 0, 0);
      }
      __builtin_amdgcn_raw_buffer_store_b128(pack_acc(a.O9[0], a.O9[1], 1.0f / l9), rO, a.l16 + so + w * 9216 + 8 * 1024, 0, 0);
    }
  }
  asm volatile("s_waitcnt vmcnt(9)\n\ts_barrier" ::: "memory");
  if (bid < 320) {
    const int ks0 = (bid / 5) * 2, nt0 = (bid % 5) * 8 + 2 * w;
#pragma unroll
    for (int ks = 0; ks < 2; ks++)
#pragma unroll
      for (int i = 0; i < 2; i++) {
        const unsigned o = (a.vb[i] & 0x3fffu) + ks * 8192;
        *(u32x4*)(ws + WS_WO + ((size_t)((ks0 + ks) * 40 + nt0 + i) * 64 + lane) * 16) = ldstr(o, o + 2048);
      }
  }
  STAMP();
}

__shared__ __attribute__((aligned(1024))) char smem2[4 * 34 * 1024];
struct OPB { u32x4 o[9], wv[5]; };
__global__ __launch_bounds__(256, 1) void oproj_kernel(const char* __restrict__ ws, u16* __restrict__ out) {
  const int w = __builtin_amdgcn_readfirstlane(threadIdx.x >> 6);
  const unsigned lane = threadIdx.x & 63;
  const int bid = blockIdx.x, xcd = bid & 7, idx = bid >> 3, b = xcd * 4 + (idx >> 3), j = idx & 7;
  const u32x4* Of = (const u32x4*)(ws + WS_O) + (size_t)(b * 128 + w) * 9 * 64 + lane;
  const u32x4* Wf = (const u32x4*)(ws + WS_WO) + (size_t)(w * 40 + 5 * j) * 64 + lane;
  f32x4 acc[9][5];
#pragma unroll
  for (int q = 0; q < 9; q++)
#pragma unroll
    for (int n = 0; n < 5; n++) acc[q][n] = f32x4{0.f, 0.f, 0.f, 0.f};
  OPB buf[4];
  auto ld = [&](OPB& d, int it) {
    const u32x4* o = Of + (size_t)it * 4 * 9 * 64;
    const u32x4* p = Wf + (size_t)it * 4 * 40 * 64;
#pragma unroll
    for (int n = 0; n < 5; n++) d.wv[n] = p[n * 64];
#pragma unroll
    for (int q = 0; q < 9; q++) d.o[q] = o[q * 64];
  };
  auto mm = [&](const OPB& d) {
#pragma unroll
    for (int q = 0; q < 9; q++)
#pragma unroll
      for (int n = 0; n < 5; n++) acc[q][n] = mfma(d.wv[n], d.o[q], acc[q][n]);
  };
  ld(buf[0], 0); ld(buf[1], 1); ld(buf[2], 2);
#pragma unroll 1
  for (int it = 0; it < 28; it += 4) {
    ld(buf[3], it + 3); __builtin_amdgcn_sched_barrier(0); mm(buf[0]); __builtin_amdgcn_sched_barrier(0);
    ld(buf[0], it + 4); __builtin_amdgcn_sched_barrier(0); mm(buf[1]); __builtin_amdgcn_sched_barrier(0);
    ld(buf[1], it + 5); __builtin_amdgcn_sched_barrier(0); mm(buf[2]); __builtin_amdgcn_sched_barrier(0);
    ld(buf[2], it + 6); __builtin_amdgcn_sched_barrier(0); mm(buf[3]); __builtin_amdgcn_sched_barrier(0);
  }
  ld(buf[3], 31); __builtin_amdgcn_sched_barrier(0);
  mm(buf[0]); mm(buf[1]); mm(buf[2]); mm(buf[3]);
  LDSP char* sm = (LDSP char*)smem2;
#pragma unroll
  for (int q = 0; q < 9; q++)
#pragma unroll
    for (int n = 0; n < 5; n++) {
      const int id = q * 5 + n, ow = id & 3;
      if (ow != w) *(LDSP f32x4*)(sm + (id * 3 + ((w - ow - 1) & 3)) * 1024 + lane * 16) = acc[q][n];
    }
  __syncthreads();
#pragma unroll
  for (int q = 0; q < 9; q++)
#pragma unroll
    for (int n = 0; n < 5; n++) {
      const int id = q * 5 + n, ow = id & 3;
      if (ow == w) {
        f32x4 s = acc[q][n];
#pragma unroll
        for (int r = 0; r < 3; r++) s += *(const LDSP f32x4*)(sm + (id * 3 + r) * 1024 + lane * 16);
        u32x2 v = {cvtpk(s[0], s[1]), cvtpk(s[2], s[3])};
        *(u32x2*)(out + (size_t)(b * SQ + q * 16 + (lane & 15)) * DM + j * 80 + n * 16 + (lane >> 4) * 4) = v;
      }
    }
}

extern "C" void kernel_launch(void* const* d_in, const int* in_sizes, int n_in, void* d_out, int out_size, void* d_ws, size_t ws_size,
                              hipStream_t stream) {
  attn_kernel<<<512, 256, 0, stream>>>((const u16*)d_in[0], (const u16*)d_in[1], (const u16*)d_in[2], (const u16*)d_in[3], (const u16*)d_in[4],
                                       (char*)d_ws);
  oproj_kernel<<<256, 256, 0, stream>>>((const char*)d_ws, (u16*)d_out);
}
```

```cpp
#include <hip/hip_runtime.h>
#include <hip/hip_bf16.h>

typedef short    s16x4 __attribute__((ext_vector_type(4)));
typedef __bf16   bf16x8 __attribute__((ext_vector_type(8)));
typedef __bf16   bf16x2 __attribute__((ext_vector_type(2)));
typedef float    f32x4 __attribute__((ext_vector_type(4)));
typedef float    f32x2 __attribute__((ext_vector_type(2)));
typedef unsigned u32x4 __attribute__((ext_vector_type(4)));
typedef unsigned u32x2 __attribute__((ext_vector_type(2)));
typedef unsigned short u16;
#define LDSP __attribute__((address_space(3)))
#ifdef __HIP_DEVICE_COMPILE__
#define NOPK __attribute__((target("no-packed-fp32-ops")))
#else
#define NOPK
#endif
#define DI __device__ __forceinline__ NOPK
#define LI NOPK __attribute__((always_inline))
#define F2U(x) __builtin_bit_cast(unsigned, (float)(x))
#define U2F(x) __builtin_bit_cast(float, (unsigned)(x))
#define FMAX __builtin_fmaxf
template <int N> struct IC { static constexpr int value = N; };

constexpr int NB = 32, SQ = 144, DM = 640, NH = 32, SK = 512, DH = 128;
constexpr float QSCALE = 0.08838834764831845f * 1.4426950408889634f;
constexpr float THR = 8.0f;

constexpr size_t WS_O  = 0;
constexpr size_t WS_WO = (size_t)NB * 128 * 9 * 1024;

constexpr unsigned L_Q9 = 65536, L_P9 = L_Q9 + 4096, L_M9 = L_P9 + 2048, L_X9 = L_M9 + 256, L_L9 = L_X9 + 256, L_XN = L_L9 + 512, L_DUM = L_XN + 3 * 2048, L_END = L_DUM + 256;

DI f32x4 mfma(u32x4 a, u32x4 b, f32x4 c) {
  return __builtin_amdgcn_mfma_f32_16x16x32_bf16(__builtin_bit_cast(bf16x8, a), __builtin_bit_cast(bf16x8, b), c, 0, 0, 0);
}
DI unsigned cvtpk(float lo, float hi) {
  f32x2 v = {lo, hi};
  return __builtin_bit_cast(unsigned, __builtin_convertvector(v, bf16x2));
}
DI float ex2(float x) { return __builtin_amdgcn_exp2f(x); }
DI float xmax(float v) {
  unsigned u = F2U(v);
  auto r = __builtin_amdgcn_permlane32_swap(u, u, false, false);
  float a = FMAX(U2F(r[0]), U2F(r[1]));
  u = F2U(a);
  auto s = __builtin_amdgcn_permlane16_swap(u, u, false, false);
  return FMAX(U2F(s[0]), U2F(s[1]));
}
DI float xsum(float v) {
  unsigned u = F2U(v);
  auto r = __builtin_amdgcn_permlane32_swap(u, u, false, false);
  float a = U2F(r[0]) + U2F(r[1]);
  u = F2U(a);
  auto s = __builtin_amdgcn_permlane16_swap(u, u, false, false);
  return U2F(s[0]) + U2F(s[1]);
}
DI u32x4 pack_nat(f32x4 t0, f32x4 t1, float sc) {
  unsigned a0 = cvtpk(t0[0] * sc, t0[1] * sc), a1 = cvtpk(t0[2] * sc, t0[3] * sc);
  unsigned b0 = cvtpk(t1[0] * sc, t1[1] * sc), b1 = cvtpk(t1[2] * sc, t1[3] * sc);
  auto r0 = __builtin_amdgcn_permlane32_swap(a0, b0, false, false);
  auto r1 = __builtin_amdgcn_permlane32_swap(a1, b1, false, false);
  auto s0 = __builtin_amdgcn_permlane16_swap(r0[0], r0[1], false, false);
  auto s1 = __builtin_amdgcn_permlane16_swap(r1[0], r1[1], false, false);
  return u32x4{s0[0], s1[0], s0[1], s1[1]};
}
DI u32x4 pack_acc(f32x4 t0, f32x4 t1, float sc) {
  return u32x4{cvtpk(t0[0] * sc, t0[1] * sc), cvtpk(t0[2] * sc, t0[3] * sc), cvtpk(t1[0] * sc, t1[1] * sc), cvtpk(t1[2] * sc, t1[3] * sc)};
}
DI unsigned scale_pk(unsigned p, float a) {
  return cvtpk(U2F(p << 16) * a, U2F(p & 0xffff0000u) * a);
}
DI float fmax8(const f32x4& a, const f32x4& b) {
  return FMAX(FMAX(FMAX(a[0], a[1]), FMAX(a[2], a[3])), FMAX(FMAX(b[0], b[1]), FMAX(b[2], b[3])));
}

#define SB() __builtin_amdgcn_sched_barrier(0)

DI u32x4 make_rsrc(const void* p, unsigned bytes) {
  const unsigned long long v = (unsigned long long)p;
  return u32x4{(unsigned)v, (unsigned)(v >> 32) & 0xffffu, bytes, 0x00020000u};
}
#define CODE_RSRC(R, K, E)                                                                                          \
  u32x4 R;                                                                                                          \
  {                                                                                                                 \
    unsigned long long pc_; unsigned n_;                                                                            \
    asm volatile("s_getpc_b64 vcc\n\ts_add_u32 vcc_lo, vcc_lo, " K "@rel32@lo+4\n\ts_addc_u32 vcc_hi, vcc_hi, " K "@rel32@hi+12\n\t" \
                 "s_mov_b64 %0, vcc\n\ts_mov_b32 %1, " E "-" K : "=s"(pc_), "=s"(n_) :: "scc", "vcc");              \
    R = make_rsrc((const void*)pc_, n_);                                                                            \
  }
#define KA "_Z11attn_kernelPKtS0_S0_S0_S0_Pc"
#define KO "_Z12oproj_kernelPKcPt"
template <int I> DI void dma1(unsigned mbase, unsigned voff, u32x4 r, unsigned soff) {
  constexpr int MO = (I >> 1) * 8192 + (I & 1) * (4096 - 128), IO = (I & 1) * 128;
  asm volatile("s_add_u32 m0, %0, %4\n\ts_nop 0\n\tbuffer_load_dwordx4 %1, %2, %3 offen offset:%5 nt lds"
               :: "s"(mbase), "v"(voff), "s"(r), "s"(soff), "i"(MO), "i"(IO) : "memory", "scc");
}
template <unsigned MO, unsigned SO, bool NT> DI void dmaq(unsigned mbase, unsigned voff, u32x4 r, unsigned sbase) {
  unsigned t;
  if constexpr (NT)
    asm volatile("s_add_u32 m0, %1, %5\n\ts_add_u32 %0, %4, %6\n\tbuffer_load_dwordx4 %2, %3, %0 offen nt lds"
                 : "=&s"(t) : "s"(mbase), "v"(voff), "s"(r), "s"(sbase), "i"(MO), "i"(SO) : "memory", "scc");
  else
    asm volatile("s_add_u32 m0, %1, %5\n\ts_add_u32 %0, %4, %6\n\tbuffer_load_dwordx4 %2, %3, %0 offen lds"
                 : "=&s"(t) : "s"(mbase), "v"(voff), "s"(r), "s"(sbase), "i"(MO), "i"(SO) : "memory", "scc");
}
template <unsigned MO, unsigned SO> DI void dmah(unsigned mbase, unsigned voff, u32x4 r) {
  unsigned t;
  asm volatile("s_add_u32 m0, %1, %4\n\ts_mov_b32 %0, %5\n\ts_mov_b32 exec_hi, 0\n\tbuffer_load_dwordx4 %2, %3, %0 offen lds\n\ts_mov_b32 exec_hi, -1"
               : "=&s"(t) : "s"(mbase), "v"(voff), "s"(r), "i"(MO), "i"(SO) : "memory", "scc");
}
constexpr int WSL[10] = {0, 1, 2, 3, 0, 1, 3, 0, 1, 3}, XSL[10] = {2, 3, 0, 1, 2, 2, 2, 2, 2, 2};
struct XS { u32x4 r; unsigned ve, vo, v9, mw; };
template <int N, int I> DI void dma_x(const XS& x) { dmaq<XSL[N] * 16384 + I * 4096, N * 128 + (I >> 1) * 16 * 1280, false>(x.mw, (I & 1) ? x.vo : x.ve, x.r, 0u); }
template <int N> DI void dma_x9(const XS& x) { dmah<L_XN + (N % 3) * 2048, N * 128>(x.mw >> 1, x.v9, x.r); }
template <int N> DI void sync_n() { SB(); asm volatile("s_waitcnt vmcnt(%0) lgkmcnt(0)\n\ts_barrier" :: "i"(N) : "memory"); SB(); }
DI void sync_lds() { SB(); asm volatile("s_waitcnt lgkmcnt(0)\n\ts_barrier" ::: "memory"); SB(); }

__shared__ __attribute__((aligned(1024))) char smem1[L_END];
DI u32x4 lds128(unsigned off) { return *(const LDSP u32x4*)((LDSP char*)smem1 + off); }
DI f32x4 lds128f(unsigned off) { return *(const LDSP f32x4*)((LDSP char*)smem1 + off); }
DI f32x2 lds64f(unsigned off) { return *(const LDSP f32x2*)((LDSP char*)smem1 + off); }
DI void sts128(unsigned off, u32x4 v) { *(LDSP u32x4*)((LDSP char*)smem1 + off) = v; }
DI void sts64(unsigned off, u32x2 v) { *(LDSP u32x2*)((LDSP char*)smem1 + off) = v; }
DI void sts64f(unsigned off, f32x2 v) { *(LDSP f32x2*)((LDSP char*)smem1 + off) = v; }
DI void sts32(unsigned off, float v) { *(LDSP float*)((LDSP char*)smem1 + off) = v; }
DI u32x4 ldstr(unsigned off, unsigned off2) {
  u32x2 a = __builtin_bit_cast(u32x2, __builtin_amdgcn_ds_read_tr16_b64_v4i16((LDSP s16x4*)((LDSP char*)smem1 + off)));
  u32x2 b = __builtin_bit_cast(u32x2, __builtin_amdgcn_ds_read_tr16_b64_v4i16((LDSP s16x4*)((LDSP char*)smem1 + off2)));
  return u32x4{a[0], a[1], b[0], b[1]};
}
#define MFS "v_mfma_f32_16x16x32_bf16 "
#define M_(D, A, B) MFS "%[" #D "], %[" #A "], %[" #B "], %[" #D "]\n\t"
#define MC(D, A, B, C) MFS "%[" #D "], %[" #A "], %[" #B "], %[" #C "]\n\t"
#define E_(D, S) "v_exp_f32 %[" #D "], %[" #S "]\n\t"
#define A_(L, P) "v_add_f32 %[" #L "], %[" #L "], %[" #P "]\n\t"
#define C_(D, X, Y) "v_cvt_pk_bf16_f32 %[" #D "], %[" #X "], %[" #Y "]\n\t"
#define X_(M, X, Y) "v_max3_f32 %[" #M "], %[" #M "], %[" #X "], %[" #Y "]\n\t"
DI void mf(f32x4& d, u32x4 a, u32x4 b) { asm volatile(M_(d, a, b) : [d] "+v"(d) : [a] "v"(a), [b] "v"(b)); }
DI void mfc(f32x4& d, u32x4 a, u32x4 b, f32x4 c) { asm volatile(MC(d, a, b, c) : [d] "=&v"(d) : [a] "v"(a), [b] "v"(b), [c] "v"(c)); }
DI void mf2(f32x4& d, f32x4& e, u32x4 a, u32x4 b, u32x4 c) {
  asm volatile(M_(d, a, b) M_(e, a, c) : [d] "+v"(d), [e] "+v"(e) : [a] "v"(a), [b] "v"(b), [c] "v"(c));
}
DI void mf3(f32x4& d, f32x4& e, f32x4& f, u32x4 a, u32x4 b, u32x4 c, u32x4 g) {
  asm volatile(M_(d, a, b) M_(e, a, c) M_(f, a, g) : [d] "+v"(d), [e] "+v"(e), [f] "+v"(f) : [a] "v"(a), [b] "v"(b), [c] "v"(c), [g] "v"(g));
}
DI void mf2z(f32x4& d, f32x4& e, u32x4 a, u32x4 b, u32x4 c) {
  asm volatile(MFS "%0, %2, %3, 0\n\t" MFS "%1, %2, %4, 0" : "=&v"(d), "=&v"(e) : "v"(a), "v"(b), "v"(c));
}
DI void mf3z(f32x4& d, f32x4& e, f32x4& f, u32x4 a, u32x4 b, u32x4 c, u32x4 g) {
  asm volatile(MFS "%0, %3, %4, 0\n\t" MFS "%1, %3, %5, 0\n\t" MFS "%2, %3, %6, 0" : "=&v"(d), "=&v"(e), "=&v"(f) : "v"(a), "v"(b), "v"(c), "v"(g));
}
DI void mfma_fence() { asm volatile("s_nop 7\n\ts_nop 1" ::: "memory"); SB(); }

struct Att {
  f32x4 O[2][8], O9[2];
  u32x4 Q[2][4];
  f32x4 negm[2], negm9, S9;
  u32x4 VH[3], p9;
  float l[2], l9;
  unsigned kb[4], vb[8], l16, m9a;
  u32x4 rA, rB;
  unsigned vA, vB, sA, sB, inc, mb, msk, top;
};
template <int I> DI void dma_s(Att& a) {
  if constexpr (I < 2) dma1<I>(a.mb, a.vA, a.rA, a.sA); else dma1<I>(a.mb, a.vB, a.rB, a.sB);
  if constexpr (I == 3) { a.sA = ((a.sA + a.inc) & a.msk) | a.top; a.sB = ((a.sB + a.inc) & a.msk) | a.top; a.mb = (a.mb + 16384u) & 0xffffu; }
}
DI void exp4(const f32x4& s, float (&p)[4]) {
#pragma unroll
  for (int j = 0; j < 4; j++) p[j] = ex2(s[j]);
}
DI float sum4(float l, const float (&p)[4]) { return (((l + p[0]) + p[1]) + p[2]) + p[3]; }

template <bool MAIN> DI void pin_state(Att& a, f32x4 (&S)[2][2], u32x4 (&PN)[2], u32x4 (&PO)[2]) {
  if constexpr (MAIN) asm volatile("" : "+v"(S[0][0]), "+v"(S[0][1]), "+v"(S[1][0]), "+v"(S[1][1]), "+v"(PN[0]), "+v"(PN[1]), "+v"(PO[0]), "+v"(PO[1]),
                                   "+v"(a.negm[0]), "+v"(a.negm[1]), "+v"(a.S9), "+v"(a.negm9), "+v"(a.p9));
  else asm volatile("" : "+v"(a.negm9), "+v"(a.p9));
}
template <bool MAIN, bool PEND9> DI void rescale(Att& a, f32x4 (&S)[2][2], u32x4 (&PN)[2], u32x4 (&PO)[2], f32x2 npr) {
  mfma_fence();
  const f32x2 pr = -npr;
  if constexpr (MAIN) {
#pragma unroll
    for (int qa = 0; qa < 2; qa++) {
      const float d = FMAX(xmax(fmax8(S[qa][0], S[qa][1])), 0.f);
      const float al = ex2(-d);
      S[qa][0] -= d; S[qa][1] -= d; a.negm[qa] -= d; a.l[qa] *= al;
#pragma unroll
      for (int i = 0; i < 8; i++) a.O[qa][i] *= al;
#pragma unroll
      for (int j = 0; j < 4; j++) { PN[qa][j] = scale_pk(PN[qa][j], al); PO[qa][j] = scale_pk(PO[qa][j], al); }
    }
  }
  const float r = -a.negm9[0];
  float rn = FMAX(r, FMAX(pr[0], pr[1]));
  if constexpr (PEND9) rn = FMAX(rn, r + xmax(FMAX(FMAX(a.S9[0], a.S9[1]), FMAX(a.S9[2], a.S9[3]))));
  const float al = ex2(r - rn), fa = ex2(pr[0] - rn), fb = ex2(pr[1] - rn);
  a.O9[0] *= al; a.O9[1] *= al; a.l9 *= al;
  if constexpr (PEND9) a.S9 -= rn - r;
  a.p9 = u32x4{scale_pk(a.p9[0], fa), scale_pk(a.p9[1], fa), scale_pk(a.p9[2], fb), scale_pk(a.p9[3], fb)};
  a.negm9 = f32x4{-rn, -rn, -rn, -rn};
  pin_state<MAIN>(a, S, PN, PO);
}
template <int W, int SLOT> DI void sm9(Att& a) {
  float p[4];
  exp4(a.S9, p);
  a.l9 = sum4(a.l9, p);
  sts64(a.l16 + (L_P9 + SLOT * 1024 + (W & 1) * 8), u32x2{cvtpk(p[0], p[1]), cvtpk(p[2], p[3])});
  sts32(a.m9a + (SLOT * 128 + (W & 1) * 4), a.negm9[0]);
}
#define PV2(I, VF, P) mf2(a.O[0][I], a.O[1][I], VF, P[0], P[1])
#define PV3(I, VF, P) mf3(a.O[0][I], a.O[1][I], a.O9[I], VF, P[0], P[1], a.p9)
#define RDV(I) ldstr(a.vb[I] + OV, a.vb[I] + OV + 2048)
#define RDK(KT, S) lds128(a.kb[S] + OK + (KT) * 2048)
#define RDQ9(S) lds128(a.l16 + (L_Q9 + ((((S) + W) & 3) << 10)))
#define QK_OUT [d] "+v"(SN[0][0]), [e] "+v"(SN[1][0]), [f] "+v"(SN[0][1]), [g] "+v"(SN[1][1])
#define QK_IN(S) [k0] "v"(K[0][S]), [k1] "v"(K[1][S]), [q0] "v"(a.Q[0][S]), [q1] "v"(a.Q[1][S])
#define PV_OUT(I, J) [d] "+v"(a.O[0][I]), [e] "+v"(a.O[1][I]), [f] "+v"(a.O[0][J]), [g] "+v"(a.O[1][J])
#define PV_IN(V0, V1) [k0] "v"(V0), [k1] "v"(V1), [q0] "v"(PO[0]), [q1] "v"(PO[1])
#define P4(N, P) [N##0] "v"(P[0]), [N##1] "v"(P[1]), [N##2] "v"(P[2]), [N##3] "v"(P[3])
#define BLK4(F0, F1, F2, F3) M_(d, k0, q0) F0 M_(e, k0, q1) F1 M_(f, k1, q0) F2 M_(g, k1, q1) F3
template <int W, bool ODD, bool LAST> DI void att_step(Att& a, const XS& xs, f32x4 (&SO)[2][2], f32x4 (&SN)[2][2], u32x4 (&PO)[2], u32x4 (&PN)[2]) {
  constexpr unsigned OV = ODD ? 16384u : 0u, OK = OV + 8192u;
  constexpr bool Q9 = ((W >> 1) == (ODD ? 0 : 1)) && !LAST, M9 = (W >> 1) == (ODD ? 1 : 0);
  constexpr int KT9 = W & 1;
  u32x4 K[2][4], QF[4], V[5];
  float p0[4], p1[4], p2[4], p3[4];
  unsigned c0, c1, c2, c3;
  sync_n<8>();
  if constexpr (!LAST) {
    K[0][0] = RDK(0, 0); K[1][0] = RDK(1, 0);
    if constexpr (Q9) QF[0] = RDQ9(0);
    K[0][1] = RDK(0, 1); K[1][1] = RDK(1, 1);
  } else { V[0] = RDV(2); V[1] = RDV(3); }
  SB();
  asm volatile(M_(d, v, q0) E_(x, sx) M_(e, v, q1) E_(y, sy)
               : [d] "+v"(a.O[0][7]), [e] "+v"(a.O[1][7]), [x] "=&v"(p0[0]), [y] "=&v"(p0[1])
               : [v] "v"(a.VH[0]), [q0] "v"(PN[0]), [q1] "v"(PN[1]), [sx] "v"(SO[0][0][0]), [sy] "v"(SO[0][0][1]));
  if constexpr (LAST) dma_x<0, 0>(xs); else dma_s<0>(a);
  if constexpr (Q9) { QF[1] = RDQ9(1); SB(); }
  asm volatile(M_(d, v, q0) E_(x, sx) M_(e, v, q1) E_(y, sy) M_(f, v, q2) E_(z, sz)
               : [d] "+v"(a.O[0][0]), [e] "+v"(a.O[1][0]), [f] "+v"(a.O9[0]), [x] "=&v"(p0[2]), [y] "=&v"(p0[3]), [z] "=&v"(p1[0])
               : [v] "v"(a.VH[1]), [q0] "v"(PN[0]), [q1] "v"(PN[1]), [q2] "v"(a.p9), [sx] "v"(SO[0][0][2]), [sy] "v"(SO[0][0][3]), [sz] "v"(SO[0][1][0]));
  SB();
  if constexpr (!LAST) { K[0][2] = RDK(0, 2); K[1][2] = RDK(1, 2); if constexpr (Q9) QF[2] = RDQ9(2); }
  else { V[2] = RDV(4); V[3] = RDV(5); }
  SB();
  asm volatile(M_(d, v, q0) E_(x, sx) M_(e, v, q1) E_(y, sy) M_(f, v, q2) E_(z, sz)
               : [d] "+v"(a.O[0][1]), [e] "+v"(a.O[1][1]), [f] "+v"(a.O9[1]), [x] "=&v"(p1[1]), [y] "=&v"(p1[2]), [z] "=&v"(p1[3])
               : [v] "v"(a.VH[2]), [q0] "v"(PN[0]), [q1] "v"(PN[1]), [q2] "v"(a.p9), [sx] "v"(SO[0][1][1]), [sy] "v"(SO[0][1][2]), [sz] "v"(SO[0][1][3]));
  SB();
  if constexpr (!LAST) {
    asm volatile(MC(d, k0, q0, n0) C_(c0, x0, x1) C_(c1, x2, x3) MC(e, k0, q1, n1) C_(c2, y0, y1) C_(c3, y2, y3)
                 MC(f, k1, q0, n0) A_(l, x0) A_(l, x1) MC(g, k1, q1, n1) A_(l, x2) A_(l, x3)
                 : [d] "=&v"(SN[0][0]), [e] "=&v"(SN[1][0]), [f] "=&v"(SN[0][1]), [g] "=&v"(SN[1][1]),
                   [c0] "=&v"(c0), [c1] "=&v"(c1), [c2] "=&v"(c2), [c3] "=&v"(c3), [l] "+v"(a.l[0])
                 : QK_IN(0), [n0] "v"(a.negm[0]), [n1] "v"(a.negm[1]), P4(x, p0), P4(y, p1));
    PN[0] = u32x4{c0, c1, c2, c3};
    if constexpr (Q9) mfc(a.S9, K[KT9][0], QF[0], a.negm9);
    SB();
    K[0][3] = RDK(0, 3); K[1][3] = RDK(1, 3); if constexpr (Q9) QF[3] = RDQ9(3);
    SB();
    dma_s<1>(a);
    asm volatile(BLK4(A_(l, y0) A_(l, y1), A_(l, y2) A_(l, y3), E_(x, sx), E_(y, sy))
                 : QK_OUT, [l] "+v"(a.l[0]), [x] "=&v"(p2[0]), [y] "=&v"(p2[1])
                 : QK_IN(1), P4(y, p1), [sx] "v"(SO[1][0][0]), [sy] "v"(SO[1][0][1]));
    if constexpr (Q9) mf(a.S9, K[KT9][1], QF[1]);
    SB();
    V[0] = RDV(2); V[1] = RDV(3);
    SB();
    asm volatile(BLK4(E_(x, sx), E_(y, sy), E_(z, sz), E_(u, su))
                 : QK_OUT, [x] "=&v"(p2[2]), [y] "=&v"(p2[3]), [z] "=&v"(p3[0]), [u] "=&v"(p3[1])
                 : QK_IN(2), [sx] "v"(SO[1][0][2]), [sy] "v"(SO[1][0][3]), [sz] "v"(SO[1][1][0]), [su] "v"(SO[1][1][1]));
    if constexpr (Q9) mf(a.S9, K[KT9][2], QF[2]);
    SB();
    V[2] = RDV(4); V[3] = RDV(5);
    SB();
    dma_s<2>(a);
    asm volatile(BLK4(E_(x, sx), E_(y, sy), A_(l, y0) A_(l, y1), A_(l, y2) A_(l, y3))
                 : QK_OUT, [l] "+v"(a.l[1]), [x] "=&v"(p3[2]), [y] "=&v"(p3[3])
                 : QK_IN(3), P4(y, p2), [sx] "v"(SO[1][1][2]), [sy] "v"(SO[1][1][3]));
    if constexpr (Q9) mf(a.S9, K[KT9][3], QF[3]);
    SB();
  } else {
    PN[0] = u32x4{cvtpk(p0[0], p0[1]), cvtpk(p0[2], p0[3]), cvtpk(p1[0], p1[1]), cvtpk(p1[2], p1[3])};
    a.l[0] = sum4(sum4(a.l[0], p0), p1);
    exp4(SO[1][0], p2); exp4(SO[1][1], p3);
    a.l[1] = sum4(a.l[1], p2);
    asm volatile("" : "+v"(PN[0]), "+v"(a.l[0]), "+v"(a.l[1]), "+v"(p3[0]), "+v"(p3[1]), "+v"(p3[2]), "+v"(p3[3]));
    dma_x<0, 1>(xs); dma_x<0, 2>(xs);
    SB();
  }
  V[4] = RDV(6);
  a.p9 = lds128(a.l16 + (L_P9 + (ODD ? 0 : 1024)));
  const f32x2 npr = lds64f(a.m9a + (ODD ? 0 : 128));
  a.VH[0] = RDV(7);
  SB();
  asm volatile(BLK4(C_(c0, x0, x1) C_(c1, x2, x3), C_(c2, y0, y1) C_(c3, y2, y3), A_(l, y0) A_(l, y1), A_(l, y2) A_(l, y3))
               : PV_OUT(2, 3), [c0] "=&v"(c0), [c1] "=&v"(c1), [c2] "=&v"(c2), [c3] "=&v"(c3), [l] "+v"(a.l[1])
               : PV_IN(V[0], V[1]), P4(x, p2), P4(y, p3));
  PN[1] = u32x4{c0, c1, c2, c3};
  SB();
  a.VH[1] = RDV(0); a.VH[2] = RDV(1);
  SB();
  if constexpr (LAST) dma_x<0, 3>(xs); else dma_s<3>(a);
  float mx = 0.f;
  if constexpr (M9) {
    asm volatile(BLK4(E_(x0, s0) E_(x1, s1), E_(x2, s2) E_(x3, s3), C_(c0, x0, x1) A_(l, x0) A_(l, x1), C_(c1, x2, x3) A_(l, x2) A_(l, x3))
                 : PV_OUT(4, 5), [x0] "=&v"(p0[0]), [x1] "=&v"(p0[1]), [x2] "=&v"(p0[2]), [x3] "=&v"(p0[3]), [c0] "=&v"(c0), [c1] "=&v"(c1), [l] "+v"(a.l9)
                 : PV_IN(V[2], V[3]), P4(s, a.S9));
    sts64(a.l16 + (L_P9 + (ODD ? 1024 : 0) + KT9 * 8), u32x2{c0, c1});
    sts32(a.m9a + ((ODD ? 128 : 0) + KT9 * 4), a.negm9[0]);
    SB();
    if constexpr (!LAST)
      asm volatile(M_(d, v, q0) X_(m, x0, x1) X_(m, x2, x3) X_(m, y0, y1) X_(m, y2, y3) M_(e, v, q1) X_(m, z0, z1) X_(m, z2, z3) X_(m, u0, u1) X_(m, u2, u3)
                   : [d] "+v"(a.O[0][6]), [e] "+v"(a.O[1][6]), [m] "+v"(mx)
                   : [v] "v"(V[4]), [q0] "v"(PO[0]), [q1] "v"(PO[1]), P4(x, SN[0][0]), P4(y, SN[1][0]), P4(z, SN[0][1]), P4(u, SN[1][1]));
    else PV2(6, V[4], PO);
  } else if constexpr (!LAST) {
    asm volatile(BLK4(X_(m, x0, x1) X_(m, x2, x3), X_(m, y0, y1) X_(m, y2, y3), X_(m, z0, z1) X_(m, z2, z3), X_(m, u0, u1) X_(m, u2, u3))
                 : PV_OUT(4, 5), [m] "+v"(mx)
                 : PV_IN(V[2], V[3]), P4(x, SN[0][0]), P4(y, SN[1][0]), P4(z, SN[0][1]), P4(u, SN[1][1]));
    asm volatile(M_(d, v, q0) X_(m, x0, x1) X_(m, x2, x3) M_(e, v, q1)
                 : [d] "+v"(a.O[0][6]), [e] "+v"(a.O[1][6]), [m] "+v"(mx)
                 : [v] "v"(V[4]), [q0] "v"(PO[0]), [q1] "v"(PO[1]), P4(x, a.S9));
  } else { PV2(4, V[2], PO); PV2(5, V[3], PO); PV2(6, V[4], PO); }
  if constexpr (LAST) sts64f(a.m9a * 4 + (L_L9 - 4 * L_M9 + W * 8), f32x2{xsum(a.l9), a.negm9[0]});
  if constexpr (ODD && !LAST) {
#pragma unroll
    for (int s = 0; s < 4; s++) a.kb[s] ^= 0x8000u;
#pragma unroll
    for (int i = 0; i < 8; i++) a.vb[i] ^= 0x8000u;
  }
  const bool rare = (mx > THR) | (npr[0] != a.negm9[0]) | (npr[1] != a.negm9[0]);
  pin_state<!LAST>(a, SN, PN, PO);
  if (__builtin_amdgcn_ballot_w64(rare) != 0) rescale<!LAST, Q9>(a, SN, PN, PO, npr);
  pin_state<!LAST>(a, SN, PN, PO);
}

struct Nxt { u32x4 rV, rN; unsigned voV, voN, sV15, sN, dN, incN; };
template <int W> DI void attention(Att& a, const Nxt& nx, const XS& xs, u32x4 (&PA)[2], u32x4 (&PB)[2]) {
  f32x4 SA[2][2], SB_[2][2];
  const f32x4 z = {0.f, 0.f, 0.f, 0.f};
  {
    sync_n<8>();
    u32x4 K[2][4], QF[4];
    constexpr int T9 = W >> 1, KT9 = W & 1;
#pragma unroll
    for (int tt = 0; tt < 2; tt++) {
      const int tl = tt ^ T9;
      const unsigned OK = tl * 8192;
      f32x4 (&S)[2][2] = tl == 0 ? SA : SB_;
#pragma unroll
      for (int s = 0; s < 4; s++) { K[0][s] = RDK(0, s); K[1][s] = RDK(1, s); if (tt == 0) QF[s] = RDQ9(s); }
      if (tt == 0) { dma_s<0>(a); dma_s<1>(a); } else { dma_s<2>(a); dma_s<3>(a); }
#pragma unroll
      for (int s = 0; s < 4; s++) {
        if (tt == 0) a.S9 = mfma(K[KT9][s], QF[s], s == 0 ? z : a.S9);
#pragma unroll
        for (int kt = 0; kt < 2; kt++) {
          S[0][kt] = mfma(K[kt][s], a.Q[0][s], s == 0 ? z : S[0][kt]);
          S[1][kt] = mfma(K[kt][s], a.Q[1][s], s == 0 ? z : S[1][kt]);
        }
      }
      if (tt == 1) sts32(a.m9a * 2 + (L_X9 - 2 * L_M9 + W * 4), xmax(FMAX(FMAX(a.S9[0], a.S9[1]), FMAX(a.S9[2], a.S9[3]))));
    }
    sync_lds();
    const f32x4 m4 = lds128f(a.m9a * 2 + (L_X9 - 2 * L_M9));
#pragma unroll
    for (int qa = 0; qa < 2; qa++) {
      const float m = xmax(FMAX(fmax8(SA[qa][0], SA[qa][1]), fmax8(SB_[qa][0], SB_[qa][1])));
      SA[qa][0] -= m; SA[qa][1] -= m; SB_[qa][0] -= m; SB_[qa][1] -= m;
      a.negm[qa] = f32x4{-m, -m, -m, -m};
      float p0[4], p1[4];
      exp4(SA[qa][0], p0); exp4(SA[qa][1], p1);
      a.l[qa] = sum4(sum4(0.f, p0), p1);
      PA[qa] = u32x4{cvtpk(p0[0], p0[1]), cvtpk(p0[2], p0[3]), cvtpk(p1[0], p1[1]), cvtpk(p1[2], p1[3])};
      PB[qa] = u32x4{0u, 0u, 0u, 0u};
    }
    const float m9 = FMAX(FMAX(m4[0], m4[1]), FMAX(m4[2], m4[3]));
    a.S9 -= m9; a.negm9 = f32x4{-m9, -m9, -m9, -m9}; a.l9 = 0.f;
    if constexpr (T9 == 0) sm9<W, 0>(a);
#pragma unroll
    for (int i = 0; i < 8; i++) { a.O[0][i] = z; a.O[1][i] = z; }
    a.O9[0] = z; a.O9[1] = z;
    a.VH[0] = PB[0]; a.VH[1] = PB[0]; a.VH[2] = PB[0]; a.p9 = PB[0];
  }
#pragma unroll 1
  for (int t = 1; t < 15; t += 2) {
    att_step<W, true, false>(a, xs, SB_, SA, PA, PB);
    if (t == 11) { a.rB = nx.rV; a.vB = nx.voV; a.sB = nx.sV15; }
    att_step<W, false, false>(a, xs, SA, SB_, PB, PA);
    if (t == 11) { a.rA = nx.rN; a.rB = nx.rN; a.vA = nx.voN; a.vB = nx.voN; a.sA = nx.sN; a.sB = nx.sN + nx.dN; a.inc = nx.incN; a.msk = 0xffffffffu; a.top = 0u; }
  }
  att_step<W, true, true>(a, xs, SB_, SA, PA, PB);
}
DI void drain(Att& a, const XS& xs, u32x4 (&PA)[2], u32x4 (&PB)[2], float& l9out) {
  f32x4 SA[2][2];
  constexpr unsigned OV = 16384u + 8192u;
  sync_lds();
  u32x4 V[8];
  V[2] = RDV(2); V[3] = RDV(3);
  const u32x4 p9n = lds128(a.l16 + (L_P9 + 1024));
  const f32x2 pr = lds64f(a.m9a + 128);
  V[4] = RDV(4); V[5] = RDV(5);
  SB();
  PV2(7, a.VH[0], PA); dma_x9<0>(xs);
  PV3(0, a.VH[1], PA);
  SB();
  V[6] = RDV(6); V[7] = RDV(7);
  SB();
  PV3(1, a.VH[2], PA); dma_x9<1>(xs);
  SB();
  V[0] = RDV(0); V[1] = RDV(1);
  const f32x4 la = lds128f(a.m9a * 4 + (L_L9 - 4 * L_M9)), lb = lds128f(a.m9a * 4 + (L_L9 - 4 * L_M9 + 16));
  SB();
  PV2(2, V[2], PB); PV2(3, V[3], PB); PV2(4, V[4], PB); PV2(5, V[5], PB);
  PV2(6, V[6], PB); PV2(7, V[7], PB); PV2(0, V[0], PB); PV2(1, V[1], PB);
  a.p9 = p9n;
  if (__builtin_amdgcn_ballot_w64((pr[0] != a.negm9[0]) | (pr[1] != a.negm9[0])) != 0) rescale<false, false>(a, SA, PA, PB, pr);
  asm volatile("s_nop 1" : "+v"(a.p9), "+v"(a.O9[0]), "+v"(a.O9[1]));
  mf(a.O9[0], V[0], a.p9); mf(a.O9[1], V[1], a.p9);
  mfma_fence();
  const float n9 = a.negm9[0];
  l9out = (la[0] * ex2(n9 - la[1]) + la[2] * ex2(n9 - la[3])) + (lb[0] * ex2(n9 - lb[1]) + lb[2] * ex2(n9 - lb[3]));
}

__global__ __launch_bounds__(256, 2) NOPK void attn_kernel(const u16* __restrict__ x, const u16* __restrict__ kc, const u16* __restrict__ vc,
                                                      const u16* __restrict__ wq, const u16* __restrict__ wo, char* __restrict__ ws) {
  const int w = __builtin_amdgcn_readfirstlane(__builtin_amdgcn_workitem_id_x() >> 6);
  const int bid = __builtin_amdgcn_workgroup_id_x(), xcd = bid & 7, idx = bid >> 3;
  unsigned lane = __builtin_amdgcn_workitem_id_x() & 63;
  {
    CODE_RSRC(rC, KA, ".Lattn_code_end");
    asm volatile("s_mov_b32 m0, %3\n\ts_nop 0\n\tbuffer_load_dword %0, %1, %2 offen lds"
                 :: "v"((lane & 31) * 128), "s"(rC), "s"((unsigned)((idx & 15) * 4 + w) * 4096u), "i"(L_DUM) : "memory");
  }
  SB();
  const int b = (xcd >> 1) * 8 + (idx & 7), hp = (xcd & 1) * 8 + (idx >> 3);
  const u32x4 rK = make_rsrc(kc, NB * NH * SK * DH * 2), rV = make_rsrc(vc, NB * NH * SK * DH * 2);
  const u32x4 rW = make_rsrc(wq, DM * NH * DH * 2), rX = make_rsrc(x, NB * SQ * DM * 2), rWo = make_rsrc(wo, DM * NH * DH * 2);
  const __amdgpu_buffer_rsrc_t rO = __builtin_amdgcn_make_buffer_rsrc((void*)(ws + WS_O), (short)0, NB * 128 * 9 * 1024 + 128 * 40 * 1024, 0x00020000);

  Att a;
  const unsigned mw = w * 1024;
#define DMA_OFFS()                                                                                                   \
  const unsigned drow = 8 * w + (lane >> 3), dch = lane & 7;                                                         \
  const unsigned voW = drow * 8192u + ((dch ^ ((((drow >> 1) & 1) | (((drow >> 3) & 1) << 1)) << 1)) << 4);           \
  XS xs;                                                                                                             \
  xs.r = rX; xs.mw = mw;                                                                                             \
  xs.ve = (b * SQ + 32 * w + (lane >> 3)) * 1280 + ((dch ^ (lane >> 4)) << 4);                                       \
  xs.vo = (xs.ve ^ 64u) + 8 * 1280;                                                                                  \
  xs.v9 = (b * SQ + 128 + 4 * w + ((lane >> 3) & 3)) * 1280 + ((dch ^ (2 * w + ((lane >> 4) & 1))) << 4);
#define DMA_W(N, J) dmaq<WSL[N] * 16384 + ((J) >> 1) * 8192 + ((J) & 1) * 4096, ((N) * 64 + ((J) >> 1) * 32) * 8192u + ((J) & 1) * 128, false>(mw, voW, rW, sw)
  {
    DMA_OFFS();
    const unsigned sw = (unsigned)hp * 256u;
    dma_x<0, 0>(xs); dma_x<0, 1>(xs); dma_x<0, 2>(xs); dma_x<0, 3>(xs); dma_x9<0>(xs);
    DMA_W(0, 0); DMA_W(0, 1); DMA_W(0, 2); DMA_W(0, 3);
    dma_x9<1>(xs);
    DMA_W(1, 0); DMA_W(1, 1); DMA_W(1, 2); DMA_W(1, 3);
    asm volatile("s_waitcnt vmcnt(5)" ::: "memory");
  }

#pragma unroll 1
  for (int hi = 0; hi < 2; hi++) {
    const int h = hp + 16 * hi;
    const unsigned sw = (unsigned)h * 256u;
    const unsigned skv0 = (unsigned)(b * NH + h) * (SK * DH * 2), rot = ((unsigned)(bid * 5 + hi * 6) % 13u), skv = skv0 + rot * 8192u;
    asm volatile("" : "+v"(lane));
    DMA_OFFS();
    const unsigned voK = drow * 256 + ((dch ^ ((drow >> 1) & 7)) << 4);
    const unsigned voV = drow * 256 + ((dch ^ (((drow >> 1) & 3) << 1)) << 4);
    const unsigned li = lane & 15, lg = lane >> 4, lq = (lane >> 2) & 3, lp = lane & 3;
    {
      unsigned wb[8];
      const unsigned row = 8 * lg + lq, fw = (((row >> 1) & 1) | (((row >> 3) & 1) << 1)) << 1;
#pragma unroll
      for (int i = 0; i < 8; i++) {
        const unsigned dt = (i + 2 * w) & 7;
        wb[i] = (dt >> 2) * 4096 + row * 128 + (((2 * (dt & 3) + (lp >> 1)) ^ fw) << 4) + 8 * (lp & 1);
      }
      const unsigned xsw = (lg ^ (li >> 1)) << 4;
      const unsigned xb0 = mw + (li >> 3) * 4096 + (li & 7) * 128 + xsw, xb1 = xb0 ^ 64u;
      const unsigned nb0 = L_XN + li * 128 + xsw, nb1 = nb0 ^ 64u;
      f32x4 acc[2][8], acc9[2];
      u32x4 WH[3], XH[3];
      auto qp_step = [&](auto nc) LI {
        constexpr int n = decltype(nc)::value;
        constexpr bool SWAP = n < 4;
        if (n == 0) sync_n<10>(); else if (n < 9) sync_n<5>(); else sync_n<4>();
        auto rd = [&](int ks, int i) LI { const unsigned o = wb[i] + WSL[n] * 16384 + ks * 8192; return ldstr(o, o + 512); };
        u32x4 X0[3], X1[3];
        auto mm = [&](int i, u32x4 wf, const u32x4 (&f)[3], bool first = false) LI {
          if (first) { if (i < 2) mf3z(acc[0][i], acc[1][i], acc9[i], wf, f[0], f[1], f[2]); else mf2z(acc[0][i], acc[1][i], wf, f[0], f[1]); }
          else { if (i < 2) mf3(acc[0][i], acc[1][i], acc9[i], wf, f[0], f[1], f[2]); else mf2(acc[0][i], acc[1][i], wf, f[0], f[1]); }
        };
        auto d1 = [&](auto jc) LI {
          constexpr int j = decltype(jc)::value;
          if constexpr (n < 9) dma_x<n + 1, j>(xs);
          else dmaq<16384 + (j >> 1) * 8192 + (j & 1) * 4096, (j < 2 ? 0 : 2 * 8192) + (j & 1) * 128, true>(mw, j < 2 ? voV : voK, j < 2 ? rV : rK, skv);
        };
        auto d2 = [&](auto jc) LI {
          constexpr int j = decltype(jc)::value;
          if constexpr (n < 8) DMA_W(n + 2, j);
          else if constexpr (n == 8) dmaq<(j >> 1) * 8192 + (j & 1) * 4096, (j >> 1) * 8192 + (j & 1) * 128, true>(mw, voK, rK, skv);
          else dmaq<32768 + (j >> 1) * 8192 + (j & 1) * 4096, (j < 2 ? 8192 : 3 * 8192) + (j & 1) * 128, true>(mw, j < 2 ? voV : voK, j < 2 ? rV : rK, skv);
        };
        u32x4 F[8], G[5];
        X0[0] = lds128(xb0 + XSL[n] * 16384); X0[1] = lds128(xb0 + XSL[n] * 16384 + 8192);
        X1[0] = lds128(xb1 + XSL[n] * 16384); X1[1] = lds128(xb1 + XSL[n] * 16384 + 8192);
        X0[2] = lds128(nb0 + (n % 3) * 2048);
        F[0] = rd(0, 0); F[1] = rd(0, 1);
        SB();
        if constexpr (n > 0) mm(7, WH[0], XH);
        if constexpr (SWAP || n == 9) d1(IC<0>{});
        SB();
        F[2] = rd(0, 2); F[3] = rd(0, 3);
        SB();
        if constexpr (n > 0) mm(0, WH[1], XH);
        if constexpr (SWAP || n == 9) d1(IC<1>{});
        SB();
        F[4] = rd(0, 4); F[5] = rd(0, 5);
        SB();
        if constexpr (n > 0) mm(1, WH[2], XH);
        asm volatile("" :: "v"(X0[0]), "v"(X0[1]), "v"(X1[0]), "v"(X1[1]) : "memory");
        if constexpr (SWAP || n == 9) d1(IC<2>{}); else d1(IC<0>{});
        mm(0, F[0], X0, n == 0);
        if constexpr (SWAP || n == 9) d1(IC<3>{}); else d1(IC<1>{});
        SB();
        F[6] = rd(0, 6); F[7] = rd(0, 7); X1[2] = lds128(nb1 + (n % 3) * 2048);
        SB();
        mm(1, F[1], X0, n == 0);
        if constexpr (!(SWAP || n == 9)) d1(IC<2>{});
        mm(2, F[2], X0, n == 0);
        if constexpr (!(SWAP || n == 9)) d1(IC<3>{});
        SB();
        G[0] = rd(1, 2); G[1] = rd(1, 3);
        SB();
        mm(3, F[3], X0, n == 0); mm(4, F[4], X0, n == 0);
        if constexpr (n < 8) dma_x9<n + 2>(xs);
        SB();
        G[2] = rd(1, 4); G[3] = rd(1, 5);
        SB();
        mm(5, F[5], X0, n == 0); mm(6, F[6], X0, n == 0); d2(IC<0>{});
        SB();
        G[4] = rd(1, 6); WH[0] = rd(1, 7);
        SB();
        mm(7, F[7], X0, n == 0); mm(2, G[0], X1); d2(IC<1>{});
        SB();
        WH[1] = rd(1, 0); WH[2] = rd(1, 1);
        SB();
        mm(3, G[1], X1); mm(4, G[2], X1); d2(IC<2>{});
        mm(5, G[3], X1); d2(IC<3>{}); mm(6, G[4], X1);
        XH[0] = X1[0]; XH[1] = X1[1]; XH[2] = X1[2];
        if constexpr (n == 9) { mm(0, WH[1], XH); mm(1, WH[2], XH); mm(7, WH[0], XH); }
      };
      qp_step(IC<0>{}); qp_step(IC<1>{}); qp_step(IC<2>{}); qp_step(IC<3>{}); qp_step(IC<4>{});
      qp_step(IC<5>{}); qp_step(IC<6>{}); qp_step(IC<7>{}); qp_step(IC<8>{}); qp_step(IC<9>{});
      mfma_fence();
      sts128(L_Q9 + w * 1024 + lane * 16, pack_nat(acc9[0], acc9[1], QSCALE));
#pragma unroll
      for (int s = 0; s < 4; s++) {
        a.Q[0][s] = pack_nat(acc[0][2 * s], acc[0][2 * s + 1], QSCALE);
        a.Q[1][s] = pack_nat(acc[1][2 * s], acc[1][2 * s + 1], QSCALE);
      }
    }
#pragma unroll
    for (int s = 0; s < 4; s++) {
      const unsigned sa = (s + w) & 3;
      a.kb[s] = (sa >> 1) * 4096 + li * 128 + (((4 * (sa & 1) + lg) ^ (li >> 1)) << 4);
    }
    {
      const unsigned row = 4 * lg + lq, fv = ((row >> 1) & 3) << 1;
#pragma unroll
      for (int i = 0; i < 8; i++) {
        const unsigned dt = (i + 2 * w) & 7;
        a.vb[i] = (dt >> 2) * 4096 + row * 128 + (((2 * (dt & 3) + (lp >> 1)) ^ fv) << 4) + 8 * (lp & 1);
      }
    }
    a.l16 = lane * 16; a.m9a = L_M9 + li * 8;
    a.rA = rV; a.vA = voV; a.sA = skv + 2 * 8192; a.rB = rK; a.vB = voK; a.sB = skv0 + ((rot + 4) & 15) * 8192; a.msk = 0x1ffffu; a.top = skv0; a.inc = 8192; a.mb = mw + 49152u;
    xs.ve |= (unsigned)hi << 30; xs.vo |= (unsigned)hi << 30; xs.v9 |= (unsigned)hi << 30;
    Nxt nx;
    nx.rV = rV; nx.voV = voV; nx.sV15 = skv0 + ((rot + 15) & 15) * 8192;
    if (hi == 0) { nx.rN = rW; nx.voN = voW; nx.sN = sw + 16u * 256u; nx.dN = 32u * 8192u; nx.incN = 64u * 8192u; }
    else {
      const unsigned t0 = (unsigned)bid & 255u, s0 = (t0 / 5) * (64u * 1280u) + (t0 % 5) * 256u;
      nx.rN = rWo; nx.voN = voV + drow * (1280 - 256); nx.dN = 32u * 1280u;
      nx.sN = bid >= 256 ? s0 : 0x20000000u;
      nx.incN = (bid >> 6) == 4 ? (unsigned)(bid / 5) * (64u * 1280u) + (unsigned)(bid % 5) * 256u - s0 : 0x20000000u;
    }
    float l9;
    u32x4 PA[2], PB[2];
    if (bid >= 256 && hi == 1) __builtin_amdgcn_s_setprio(3); else __builtin_amdgcn_s_setprio(2);
    switch (w) {
      case 0: attention<0>(a, nx, xs, PA, PB); break;
      case 1: attention<1>(a, nx, xs, PA, PB); break;
      case 2: attention<2>(a, nx, xs, PA, PB); break;
      default: attention<3>(a, nx, xs, PA, PB); break;
    }
    drain(a, xs, PA, PB, l9);
    __builtin_amdgcn_s_setprio(0);
    {
      const unsigned so = (unsigned)(b * 128 + h * 4) * 9216u;
#pragma unroll
      for (int qa = 0; qa < 2; qa++) {
        const float inv = 1.0f / xsum(a.l[qa]);
#pragma unroll
        for (int u = 0; u < 4; u++)
          __builtin_amdgcn_raw_buffer_store_b128(pack_acc(a.O[qa][2 * u], a.O[qa][2 * u + 1], inv), rO,
                                                 a.l16 + so + ((u + w) & 3) * 9216 + (2 * w + qa) * 1024, 0, 16);
      }
      __builtin_amdgcn_raw_buffer_store_b128(pack_acc(a.O9[0], a.O9[1], 1.0f / l9), rO, a.l16 + so + w * 9216 + 8 * 1024, 0, 16);
    }
  }
  asm volatile("s_waitcnt vmcnt(9)\n\ts_barrier" ::: "memory");
  {
    CODE_RSRC(rC, KO, ".Loproj_code_end");
    asm volatile("s_mov_b32 m0, %3\n\ts_nop 4\n\tbuffer_load_dword %0, %1, %2 offen lds"
                 :: "v"((lane & 31) * 128), "s"(rC), "s"(mw * 4 + (unsigned)(idx & 7) * 16384u), "i"(L_DUM) : "memory");
  }
#pragma unroll 1
  for (int tl = bid - 256; tl >= 0 && tl < 320; tl += 256) {
    const int ks0 = (tl / 5) * 2, nt0 = (tl % 5) * 8 + 2 * w;
#pragma unroll
    for (int ks = 0; ks < 2; ks++)
#pragma unroll
      for (int i = 0; i < 2; i++) {
        const unsigned o = (a.vb[i] & 0x3fffu) + ks * 8192 + (tl >> 8) * 16384;
        __builtin_amdgcn_raw_buffer_store_b128(ldstr(o, o + 2048), rO, lane * 16, (unsigned)WS_WO + (unsigned)((ks0 + ks) * 40 + nt0 + i) * 1024u, 16);
      }
  }
  asm volatile("s_waitcnt vmcnt(0)\n.Lattn_code_end:" ::: "memory");
}

constexpr unsigned WS_Q = NB * 128 * 9 * 1024 + 128 * 40 * 1024;
__shared__ __attribute__((aligned(1024))) char smem2[140 * 1024];
struct OPB { u32x4 o[9], wv[5]; };
__global__ __launch_bounds__(256, 1) NOPK void oproj_kernel(const char* __restrict__ ws, u16* __restrict__ out) {
  const int w = __builtin_amdgcn_readfirstlane(__builtin_amdgcn_workitem_id_x() >> 6);
  const unsigned lane = __builtin_amdgcn_workitem_id_x() & 63;
  const int bid = __builtin_amdgcn_workgroup_id_x(), xcd = bid & 7, idx = bid >> 3, b = xcd * 4 + (idx >> 3), j = idx & 7;
  const __amdgpu_buffer_rsrc_t rS = __builtin_amdgcn_make_buffer_rsrc((void*)ws, (short)0, WS_Q, 0x00020000);
  const unsigned so = (unsigned)(b * 128 + w) * 9216u, sv = (unsigned)WS_WO + (unsigned)(w * 40 + 5 * j) * 1024u, l16 = lane * 16;
  unsigned cp;
  CODE_RSRC(rC, KO, ".Loproj_code_end");
  asm volatile("buffer_load_dword %0, %1, %2, %3 offen" : "=v"(cp) : "v"((lane & 31) * 128), "s"(rC), "s"((j * 4 + w) * 4096) : "memory");
  SB();
  f32x4 acc[9][5];
#pragma unroll
  for (int i = 0; i < 45; i++) acc[i / 5][i % 5] = f32x4{0.f, 0.f, 0.f, 0.f};
  OPB buf[4];
  auto ld1 = [&](OPB& d, int it, int r) LI {
    if (r < 5) d.wv[r] = __builtin_amdgcn_raw_buffer_load_b128(rS, l16 + (r & 3) * 1024, sv + it * 163840 + (r >> 2) * 4096, 0);
    else d.o[r - 5] = __builtin_amdgcn_raw_buffer_load_b128(rS, l16 + ((r - 5) & 1) * 1024, so + it * 36864 + (r == 13 ? 4 : (((r - 5) >> 1) + w) & 3) * 2048, 0);
  };
  auto mmld = [&](const OPB& c, OPB& d, int it, bool ld) LI {
#pragma unroll
    for (int i = 0; i < 45; i++) {
      asm volatile(M_(d, a, b) : [d] "+a"(acc[i / 5][i % 5]) : [a] "v"(c.wv[i % 5]), [b] "v"(c.o[i / 5]));
      if (ld && i % 3 == 2 && i < 42) { SB(); ld1(d, it, i / 3); SB(); }
    }
  };
#pragma unroll
  for (int i = 0; i < 42; i++) ld1(buf[i / 14], i / 14, i % 14);
  SB();
  asm volatile("s_nop 3");
#pragma unroll 1
  for (int it = 0; it < 28; it += 4) {
    mmld(buf[0], buf[3], it + 3, true); mmld(buf[1], buf[0], it + 4, true); mmld(buf[2], buf[1], it + 5, true); mmld(buf[3], buf[2], it + 6, true);
  }
  mmld(buf[0], buf[3], 31, true);
  mmld(buf[1], buf[0], 0, false); mmld(buf[2], buf[0], 0, false); mmld(buf[3], buf[0], 0, false);
  asm volatile("" :: "v"(cp));
  mfma_fence();
  LDSP char* sm = (LDSP char*)smem2;
#pragma unroll
  for (int i = 10; i < 40; i++) *(LDSP f32x4*)(sm + ((i / 10 + w) & 3) * 30720 + l16 + (i - 10) * 1024) = acc[i / 5][i % 5];
#pragma unroll
  for (int n = 0; n < 5; n++) *(LDSP f32x4*)(sm + w * 5120 + l16 + (120 + n) * 1024) = acc[8][n];
  asm volatile("s_waitcnt lgkmcnt(0)\n\ts_barrier" ::: "memory");
  u16* op = out + (size_t)(b * SQ + (lane & 15)) * DM + j * 80 + (lane >> 4) * 4;
  auto ninth = [&](int n) LI {
    f32x4 t = *(const LDSP f32x4*)(sm + n * 1024 + l16 + 120 * 1024);
#pragma unroll
    for (int r = 1; r < 4; r++) t += *(const LDSP f32x4*)(sm + n * 1024 + l16 + (120 + 5 * r) * 1024);
    *(u32x2*)(op + 128 * DM + n * 16) = u32x2{cvtpk(t[0], t[1]), cvtpk(t[2], t[3])};
  };
  ninth(w);
#pragma unroll
  for (int i = 0; i < 10; i++) {
    f32x4 t = acc[i / 5][i % 5];
#pragma unroll
    for (int r = 0; r < 3; r++) t += *(const LDSP f32x4*)(sm + w * 30720 + l16 + (r * 10 + i) * 1024);
    *(u32x2*)(op + (2 * w + i / 5) * 16 * DM + (i % 5) * 16) = u32x2{cvtpk(t[0], t[1]), cvtpk(t[2], t[3])};
  }
  if (w == 0) ninth(4);
  asm volatile(".Loproj_code_end:");
}

extern "C" void kernel_launch(void* const* d_in, const int* in_sizes, int n_in, void* d_out, int out_size, void* d_ws, size_t ws_size,
                              hipStream_t stream) {
  attn_kernel<<<512, 256, 0, stream>>>((const u16*)d_in[0], (const u16*)d_in[1], (const u16*)d_in[2], (const u16*)d_in[3], (const u16*)d_in[4],
                                       (char*)d_ws);
  oproj_kernel<<<256, 256, 0, stream>>>((const char*)d_ws, (u16*)d_out);
}
```
